```python
import torch

def _softmax(x, axis=-1):
    return torch.softmax(x, dim=axis)
def _gelu(x):
    return torch.nn.functional.gelu(x, approximate="tanh")
def _silu(x):
    return torch.nn.functional.silu(x)
def _sigmoid(x):
    return torch.sigmoid(x)
def _layernorm(x, axis=-1, eps=1e-5):
    m = x.mean(dim=axis, keepdim=True)
    v = x.var(dim=axis, keepdim=True, unbiased=False)
    return (x - m) / torch.sqrt(v + eps)
def _scatter_add(tgt, idx, src, axis=0):
    return tgt.clone().index_add_(axis, idx, src)
def _segment_sum(src, seg, n):
    out = torch.zeros((n,) + src.shape[1:], dtype=src.dtype)
    return out.index_add_(0, seg, src)
def _pad(x, pw):
    flat = tuple(v for pair in reversed(pw) for v in pair)
    return torch.nn.functional.pad(x, flat)

kernel_name = 'standalone__linear_kernel_performer__prompt'

def setup_inputs():

    _w = (lambda a: (lambda t: t.to(torch.bfloat16) if t.is_floating_point() else t)(torch.as_tensor(a)))
    B, H, S, D = 1, 32, 2048, 128
    M = D
    return {
        "q": _w((lambda *s: torch.randn(*s, dtype=torch.bfloat16) * 0.05)(B, H, S, D)),
        "k": _w((lambda *s: torch.randn(*s, dtype=torch.bfloat16) * 0.8)(B, H, S, D)),
        "v": _w((lambda *s: torch.randn(*s, dtype=torch.bfloat16) * 0.8)(B, H, S, D)),
        "proj": _w((lambda *s: torch.randn(*s, dtype=torch.bfloat16) * 0.8)(M, D)),
    }

def reference(q, k, v, proj):
    B, H, S, D = q.shape
    M = proj.shape[0]
    scale = D ** -0.25
    qs = q * scale
    ks = k * scale
    qw = torch.einsum("bhsd,md->bhsm", qs, proj)
    kw = torch.einsum("bhsd,md->bhsm", ks, proj)
    qn = torch.einsum("bhsd,bhsd->bhs", qs, qs)[..., None] * 0.5
    kn = torch.einsum("bhsd,bhsd->bhs", ks, ks)[..., None] * 0.5
    inv_sqrt_m = M ** -0.5
    phi_q = torch.exp(qw - qn) * inv_sqrt_m
    phi_k = torch.exp(kw - kn) * inv_sqrt_m
    kv = torch.einsum("bhsm,bhsd->bhmd", phi_k, v)
    ksum = torch.einsum("bhsm->bhm", phi_k)
    num = torch.einsum("bhsm,bhmd->bhsd", phi_q, kv)
    den = torch.einsum("bhsm,bhm->bhs", phi_q, ksum)[..., None] + 1e-6
    return num / den
```

```cpp
#include <hip/hip_runtime.h>
#include <hip/hip_bf16.h>
constexpr int H=32,S=2048,D=128,M=128;
__device__ inline float b2f(unsigned short x){ return __uint_as_float(((unsigned)x)<<16); }
__global__ void k_phi(const unsigned short* x, const unsigned short* proj, float* phi){
  __shared__ float xs[D];
  __shared__ float red[D];
  int row = blockIdx.x; int t = threadIdx.x;
  float scale = 0.29730177875068026f;
  float v = b2f(x[(size_t)row*D+t])*scale;
  xs[t]=v; red[t]=v*v;
  __syncthreads();
  float n=0;
#pragma unroll 4
 for(int i=0;i<D;i++) n+=red[i];
  n*=0.5f;
  float a=0;
#pragma unroll 4
 for(int d=0;d<D;d++) a+= xs[d]*b2f(proj[t*D+d]);
  phi[(size_t)row*M+t]=expf(a-n)*0.08838834764831845f;
}
__global__ void k_kv(const float* phik, const unsigned short* v, float* kv, float* ksum){
  int h=blockIdx.x/M, m=blockIdx.x%M; int d=threadIdx.x;
  float a=0,s=0;
  for(int i=0;i<S;i++){ float p=phik[((size_t)h*S+i)*M+m]; a+=p*b2f(v[((size_t)h*S+i)*D+d]); s+=p; }
  kv[((size_t)h*M+m)*D+d]=a; if(d==0) ksum[h*M+m]=s;
}
__global__ void k_out(const float* phiq, const float* kv, const float* ksum, __hip_bfloat16* out){
  int row=blockIdx.x; int h=row/S; int d=threadIdx.x;
  float a=0,s=0;
  for(int m=0;m<M;m++){ float p=phiq[(size_t)row*M+m]; a+=p*kv[((size_t)h*M+m)*D+d]; s+=p*ksum[h*M+m]; }
  out[(size_t)row*D+d]=__float2bfloat16(a/(s+1e-6f));
}
extern "C" void kernel_launch(void* const* d_in, const int* in_sizes, int n_in,
                              void* d_out, int out_size, void* d_ws, size_t ws_size,
                              hipStream_t stream) {
  const unsigned short* q=(const unsigned short*)d_in[0];
  const unsigned short* k=(const unsigned short*)d_in[1];
  const unsigned short* v=(const unsigned short*)d_in[2];
  const unsigned short* proj=(const unsigned short*)d_in[3];
  float* phiq=(float*)d_ws; float* phik=phiq+(size_t)H*S*M; float* kv=phik+(size_t)H*S*M; float* ksum=kv+H*M*D;
  k_phi<<<H*S,128,0,stream>>>(q,proj,phiq);
  k_phi<<<H*S,128,0,stream>>>(k,proj,phik);
  k_kv<<<H*M,128,0,stream>>>(phik,v,kv,ksum);
  k_out<<<H*S,128,0,stream>>>(phiq,kv,ksum,(__hip_bfloat16*)d_out);
}
```

```cpp
#include <hip/hip_runtime.h>
#include <hip/hip_bf16.h>
#include <stdint.h>

typedef __attribute__((ext_vector_type(8))) __bf16 bf16x8;
typedef __attribute__((ext_vector_type(2))) __bf16 bf16x2;
typedef __attribute__((ext_vector_type(4))) short s16x4;
typedef __attribute__((ext_vector_type(8))) short s16x8;
typedef __attribute__((ext_vector_type(16))) float f32x16;
typedef __attribute__((ext_vector_type(2))) float f32x2;
typedef __attribute__((ext_vector_type(4))) float f32x4;
typedef __attribute__((ext_vector_type(4))) unsigned u32x4;
typedef __attribute__((ext_vector_type(2))) unsigned u32x2;
typedef unsigned long long u64;
#define LAS __attribute__((address_space(3)))
#define GAS __attribute__((address_space(1)))
#define DI __device__ __forceinline__

#define LOAD_NT 0
#define STORE_MODE 0
#define WIN0 10
#define WIN1 8
#define QWIN 5
#define STAMPS 0

constexpr int NH = 32, NS = 2048, ND = 128;
constexpr int ROWB = ND * 2;
constexpr unsigned L_K = 0;
constexpr unsigned L_V = 65536;
constexpr unsigned L_MISC = 65536 + 15 * 4096;
constexpr unsigned L_LQ = L_MISC;
constexpr unsigned L_KP = L_MISC + 64;
constexpr unsigned L_KS = L_MISC + 256;
constexpr unsigned L_KN = L_MISC + 1024;
constexpr unsigned L_P = 131072;
constexpr unsigned L_END = 163840;
constexpr unsigned W_GEN = 0;
constexpr unsigned W_F1 = 256;
constexpr unsigned W_KS = 1024;
constexpr unsigned W_R = 8192;
constexpr unsigned W_P1 = 8192 + 32768;
constexpr unsigned W_HEAD = W_P1 + 262144;
constexpr unsigned W_STAMP = NH * W_HEAD;

enum { KP_G2B = 1, KP_KSUM = 2, KP_KV = 3 };

constexpr float C1 = 0.29730177875068026f * 1.4426950408889634f;
constexpr float C2 = 0.5f * 0.08838834764831845f * 1.4426950408889634f;
constexpr float C3 = 3.5f;
constexpr unsigned SPIN_LDS = 1u << 20, SPIN_GLB = 1u << 16;
constexpr unsigned POISON = 0xFFFFFFFFu;

DI f32x16 mfma(bf16x8 a, bf16x8 b, f32x16 c) { return __builtin_amdgcn_mfma_f32_32x32x16_bf16(a, b, c, 0, 0, 0); }
DI unsigned pk(float a, float b) { f32x2 v = {a, b}; return __builtin_bit_cast(unsigned, __builtin_convertvector(v, bf16x2)); }
DI float dot2(unsigned a, unsigned b, float c) {
    return __builtin_amdgcn_fdot2_f32_bf16(__builtin_bit_cast(bf16x2, a), __builtin_bit_cast(bf16x2, b), c, false);
}
DI float bflo(unsigned x) { return __uint_as_float(x << 16); }
DI float bfhi(unsigned x) { return __uint_as_float(x & 0xFFFF0000u); }
DI bf16x8 asbf(u32x4 x) { return __builtin_bit_cast(bf16x8, x); }
DI u32x4 lds128(unsigned a) { return *(const LAS u32x4*)(size_t)a; }
DI void lds128w(unsigned a, u32x4 v) { *(LAS u32x4*)(size_t)a = v; }
DI void cbar() { asm volatile("" ::: "memory"); }
DI void wgbar() { asm volatile("s_waitcnt lgkmcnt(0)\n\ts_barrier" ::: "memory"); }
DI void sbar() { __builtin_amdgcn_sched_barrier(0); }
DI unsigned opaque(unsigned x) { asm volatile("" : "+v"(x)); return x; }
DI float ex2(float x) { return __builtin_amdgcn_exp2f(x); }
template <int CTRL> DI float dpp_add(float x) {
    return x + __uint_as_float(__builtin_amdgcn_update_dpp(0u, __float_as_uint(x), CTRL, 0xF, 0xF, true));
}
DI void pin8(unsigned (&x)[8]) {
    asm volatile("" : "+v"(x[0]), "+v"(x[1]), "+v"(x[2]), "+v"(x[3]), "+v"(x[4]), "+v"(x[5]), "+v"(x[6]), "+v"(x[7]));
}
template <int N> DI void vmwait() { asm volatile("s_waitcnt vmcnt(%0)" ::"n"(N) : "memory"); }

DI void glds(const char* sbase, unsigned voff, unsigned lds) {
    unsigned keep;
    asm volatile("s_mov_b32 %0, m0\n\ts_mov_b32 m0, %3\n\ts_nop 0\n\tglobal_load_lds_dwordx4 %1, %2\n\ts_mov_b32 m0, %0"
                 : "=&s"(keep) : "v"(voff), "s"(sbase), "s"(lds) : "memory");
}

DI void prog_set(unsigned addr, unsigned v) { cbar(); *(volatile LAS unsigned*)(size_t)addr = v; cbar(); }
DI void prog_wait(unsigned addr4, unsigned need) {
    for (unsigned s = 0; s < SPIN_LDS; ++s) {
        const u32x4 v = *(volatile LAS u32x4*)(size_t)addr4;
        const unsigned m = min(min(v.x, v.y), min(v.z, v.w));
        if (__builtin_amdgcn_readfirstlane(m) >= need) break;
    }
    cbar();
}
DI u64 mix64(u64 x) {
    x += 0x9E3779B97F4A7C15ull; x = (x ^ (x >> 30)) * 0xBF58476D1CE4E5B9ull; x = (x ^ (x >> 27)) * 0x94D049BB133111EBull;
    return x ^ (x >> 31);
}
#define STAMP(i) do { } while (0)
#define STAMP_OUT() do { } while (0)

constexpr int I_K = 8, I_VA = 24, I_VB = 32, I_Q = 40, I_END = 56, QA_END = I_Q + 8;
constexpr int NGRP = 14;
constexpr int GRP[NGRP] = {8, 12, 16, 20, 24, 26, 28, 30, 32, 34, 36, 38, 39, 40};
constexpr int target(int c) {
    int t = c + (c < I_VA ? WIN0 : (c < I_VB + 2 ? WIN1 : QWIN));
    if (c < 30 && t > I_Q - 1) t = I_Q - 1;
    return t < I_END ? t : I_END;
}
constexpr int issued_at(int g) {
    int n = WIN0;
    for (int j = 0; j <= g; ++j) n = target(GRP[j]) > n ? target(GRP[j]) : n;
    return n;
}
constexpr int issued_q(int c) {
    int n = issued_at(NGRP - 1);
    for (int j = I_Q; j <= c; ++j) n = target(j) > n ? target(j) : n;
    return n;
}
constexpr int STOP = issued_q(QA_END);

template <int I> struct IC { static constexpr int v = I; };
template <int B, int E, class F> DI void sfor(F&& f) { if constexpr (B < E) { f(IC<B>{}); sfor<B + 1, E>(f); } }

__global__ __launch_bounds__(512, 2) void performer_kernel(const char* __restrict__ gq, const char* __restrict__ gk,
                                                           const char* __restrict__ gv, const char* __restrict__ gp,
                                                           char* go, char* ws) {
    __shared__ __attribute__((aligned(16))) char smem[L_END];
    const int lane = threadIdx.x & 63;
    const int wave = __builtin_amdgcn_readfirstlane(threadIdx.x >> 6);
    const int b = blockIdx.x, head = (b & 7) * 4 + (b >> 6), c = (b >> 3) & 7;
    const size_t rowoff = ((size_t)head * NS + c * 256) * ROWB;
    const unsigned sb = (unsigned)(size_t)(LAS char*)smem;
    const int r = lane & 31, h = lane >> 5;
    unsigned fa[8];
#pragma unroll
    for (int ks = 0; ks < 8; ++ks) fa[ks] = sb + r * 256 + 16 * ((2 * ks + h) ^ (r & 15));

    STAMP(0);
    if (wave >= 4) {
        const int qw = wave - 4;
        const char* kb = gk + rowoff; const char* vb = gv + rowoff; const char* qb = gq + rowoff + qw * 64 * ROWB;
        const char* pb = gp + qw * 32 * ROWB;
        unsigned qo[4];
#pragma unroll
        for (int i = 0; i < 4; ++i) { const int row = 4 * i + (lane >> 4); qo[i] = row * 256 + 16 * ((lane & 15) ^ (row & 15)); }
        unsigned ko[2];
#pragma unroll
        for (int i = 0; i < 2; ++i) { const int row = 8 * qw + 4 * i + (lane >> 4); ko[i] = row * 256 + 16 * ((lane & 15) ^ (row & 15)); }
        const int vrow = 8 * qw + (lane >> 3);
        const unsigned vo = vrow * 256 + 16 * ((lane & 7) ^ (((vrow >> 1) & 1) << 2));
        auto issue = [&](auto ii) {
            constexpr int i = ii.v;
            if constexpr (i < I_K) glds(pb + (i >> 2) * 4096, qo[i & 3], sb + L_P + qw * 8192 + i * 1024);
            else if constexpr (i < I_VA) { constexpr int t = (i - I_K) >> 1, j = (i - I_K) & 1; glds(kb + t * 8192, ko[j], sb + L_K + t * 8192 + (2 * qw + j) * 1024); }
            else if constexpr (i < I_VB) { constexpr int t = i - I_VA; glds(vb + t * 8192, vo, sb + L_V + t * 4096 + qw * 1024); }
            else if constexpr (i < I_Q) { constexpr int t = i - I_VB; glds(vb + t * 8192 + 128, vo, sb + L_V + (t < 7 ? 8 + t : 0) * 4096 + qw * 1024); }
            else { constexpr int j = i - I_Q; glds(qb + (j >> 2) * 4096, qo[j & 3], sb + L_K + (2 * qw) * 8192 + j * 1024); }
        };
        const unsigned nrd = sb + L_K + qw * 2048 + lane * 32;
        auto norms = [&](auto jj) {
            constexpr int j = jj.v;
            u32x4 x[2][2];
#pragma unroll
            for (int n = 0; n < 2; ++n) { x[n][0] = lds128(nrd + (2 * j + n) * 8192); x[n][1] = lds128(nrd + (2 * j + n) * 8192 + 16); }
            float s[2][2];
#pragma unroll
            for (int n = 0; n < 2; ++n) { s[n][0] = dot2(x[n][0].x, x[n][0].x, 0.f); s[n][1] = dot2(x[n][0].y, x[n][0].y, 0.f); }
#pragma unroll
            for (int n = 0; n < 2; ++n) { s[n][0] = dot2(x[n][0].z, x[n][0].z, s[n][0]); s[n][1] = dot2(x[n][0].w, x[n][0].w, s[n][1]); }
#pragma unroll
            for (int n = 0; n < 2; ++n) { s[n][0] = dot2(x[n][1].x, x[n][1].x, s[n][0]); s[n][1] = dot2(x[n][1].y, x[n][1].y, s[n][1]); }
#pragma unroll
            for (int n = 0; n < 2; ++n) { s[n][0] = dot2(x[n][1].z, x[n][1].z, s[n][0]); s[n][1] = dot2(x[n][1].w, x[n][1].w, s[n][1]); }
            float n0 = s[0][0] + s[0][1], n1 = s[1][0] + s[1][1];
            n0 = dpp_add<0xB1>(n0); n1 = dpp_add<0xB1>(n1);
            float n = ((lane ^ (lane >> 2)) & 1) ? n1 : n0;
            n = dpp_add<0x4E>(n); n = dpp_add<0x141>(n);
            const float e = -(C2 * n + C3);
            const unsigned p0 = pk(e, 0.f);
            const float e1 = e - bflo(p0);
            const unsigned p1 = pk(e1, 0.f);
            const float e2 = e1 - bflo(p1);
            const u32x2 ext = {(p0 & 0xFFFFu) | (p1 << 16), pk(e2, 0.f)};
            if ((lane & 6) == 0) *(LAS u32x2*)(size_t)(sb + L_KN + ((2 * j + (lane & 1)) * 32 + 8 * qw + (lane >> 3)) * 8) = ext;
        };
        sfor<0, WIN0>(issue);
        sfor<0, NGRP>([&](auto gg) {
            constexpr int g = gg.v, c = GRP[g], n0 = g == 0 ? WIN0 : issued_at(g - 1);
            vmwait<n0 - c>();
            sfor<n0, issued_at(g)>(issue);
            wgbar();
            if constexpr (g >= 1 && g <= 4) norms(IC<g - 1>{});
            if constexpr (g == 0) STAMP(11);
            if constexpr (g >= 1 && g <= 4) STAMP(11 + g);
            if constexpr (g == 4) STAMP(1);
            if constexpr (g == 8) STAMP(2);
            if constexpr (g == NGRP - 1) STAMP(3);
        });
        sfor<I_Q + 1, QA_END + 1>([&](auto cc) {
            constexpr int c = cc.v, n0 = issued_q(c - 1);
            vmwait<n0 - c>();
            sfor<n0, issued_q(c)>(issue);
        });
        STAMP(4);

        unsigned phq[2][4][8];
        unsigned fp[8];
#pragma unroll
        for (int ks = 0; ks < 8; ++ks) fp[ks] = opaque(fa[ks] + L_P);
        auto gemm3 = [&](auto tile) {
            u32x4 qf[8];
#pragma unroll
            for (int ks = 0; ks < 8; ++ks) qf[ks] = lds128(fa[ks] + L_K + (2 * qw + tile.v) * 8192);
            u32x4 pa[4];
#pragma unroll
            for (int g = 0; g < 3; ++g) pa[g] = lds128(fp[g]);
            float n0 = 0.f, n1 = 0.f;
#pragma unroll
            for (int ks = 0; ks < 8; ++ks) {
                n0 = dot2(qf[ks].x, qf[ks].x, n0); n1 = dot2(qf[ks].y, qf[ks].y, n1);
                n0 = dot2(qf[ks].z, qf[ks].z, n0); n1 = dot2(qf[ks].w, qf[ks].w, n1);
            }
            const float n = n0 + n1;
            const auto sw = __builtin_amdgcn_permlane32_swap(__float_as_uint(n), __float_as_uint(n), false, false);
            const float negn = -(C2 * (__uint_as_float(sw[0]) + __uint_as_float(sw[1])) + C3);
            f32x16 ci;
#pragma unroll
            for (int i = 0; i < 16; ++i) ci[i] = negn;
            f32x16 x[3];
            sbar();
            sfor<0, 42>([&](auto ss) {
                constexpr int s = ss.v;
                if constexpr (s + 3 < 32) pa[(s + 3) & 3] = lds128(fp[(s + 3) & 7] + ((s + 3) >> 3) * 8192);
                if constexpr (s < 32) {
                    constexpr int mt = s >> 3, ks = s & 7;
                    x[mt % 3] = mfma(asbf(pa[s & 3]), asbf(qf[ks]), ks == 0 ? ci : x[mt % 3]);
                }
                if constexpr (s >= 10) {
                    constexpr int mt = (s - 10) >> 3, k = (s - 10) & 7;
                    phq[tile.v][mt][k] = pk(ex2(x[mt % 3][2 * k]), ex2(x[mt % 3][2 * k + 1]));
                    asm volatile("" : "+v"(phq[tile.v][mt][k]));
                }
                if constexpr (tile.v == 0 && (s & 3) == 1 && STOP + (s >> 2) < I_END) issue(IC<STOP + (s >> 2)>{});
                sbar();
            });
        };
        static_assert(I_END - STOP <= 11, "tile b's loads must fit into tile a's slots");
        gemm3(IC<0>{});
        STAMP(5);
        vmwait<0>();
        gemm3(IC<1>{});
        STAMP(6);
        prog_wait(sb + L_KP, KP_KSUM);
        float rden[2];
        {
            f32x16 d0 = {}, d1 = {};
#pragma unroll
            for (int mt = 0; mt < 4; ++mt)
#pragma unroll
                for (int ks = 0; ks < 2; ++ks) {
                    const bf16x8 a = asbf(lds128(sb + L_KS + ((mt * 2 + ks) * 2 + h) * 16));
                    const u32x4 b0 = {phq[0][mt][4 * ks], phq[0][mt][4 * ks + 1], phq[0][mt][4 * ks + 2], phq[0][mt][4 * ks + 3]};
                    const u32x4 b1 = {phq[1][mt][4 * ks], phq[1][mt][4 * ks + 1], phq[1][mt][4 * ks + 2], phq[1][mt][4 * ks + 3]};
                    d0 = mfma(a, asbf(b0), d0); d1 = mfma(a, asbf(b1), d1);
                }
            rden[0] = __builtin_amdgcn_rcpf(d0[0] + 1e-6f); rden[1] = __builtin_amdgcn_rcpf(d1[0] + 1e-6f);
            asm volatile("" : "+v"(rden[0]), "+v"(rden[1]));
        }
        STAMP(7);
        prog_wait(sb + L_KP, KP_KV);
        STAMP(8);
        char* ob = go + rowoff + qw * 64 * ROWB;
        {
            const unsigned kvb = opaque(sb + L_V + lane * 16);
            const unsigned stw = opaque(sb + L_K + 2 * qw * 8192 + r * 256 + 16 * (r & 15) + 8 * h);
            const unsigned str = opaque(sb + L_K + 2 * qw * 8192 + (lane >> 4) * 256 + 16 * ((lane & 15) ^ (lane >> 4)));
            auto afrag = [&](int g) { const int dt = (g >> 3) & 3, mt = (g >> 1) & 3, ks = g & 1;
                                      return lds128(kvb + (dt >> 1) * 16384 + (4 * mt + 2 * (dt & 1) + ks) * 1024); };
            u32x4 pa[4];
#pragma unroll
            for (int g = 0; g < 3; ++g) pa[g] = afrag(g);
            f32x16 acc[3];
            u32x4 ob4[2];
            sbar();
            sfor<0, 64 + 10 + 18>([&](auto ss) {
                constexpr int s = ss.v;
                if constexpr (s + 3 < 64) pa[(s + 3) & 3] = afrag(s + 3);
                if constexpr (s < 64) {
                    constexpr int tile = s >> 5, e = s >> 3, mt = (s >> 1) & 3, ks = s & 1;
                    const u32x4 bq = {phq[tile][mt][4 * ks], phq[tile][mt][4 * ks + 1], phq[tile][mt][4 * ks + 2], phq[tile][mt][4 * ks + 3]};
                    if constexpr ((s & 7) == 0) acc[e % 3] = mfma(asbf(pa[s & 3]), asbf(bq), f32x16{});
                    else acc[e % 3] = mfma(asbf(pa[s & 3]), asbf(bq), acc[e % 3]);
                }
                if constexpr (s >= 10 && s < 74 && ((s - 10) & 1) == 0) {
                    constexpr int e = (s - 10) >> 3, g = ((s - 10) & 7) >> 1, tile = e >> 2, dt = e & 3;
                    const float rd = rden[tile];
                    const u32x2 o = {pk(acc[e % 3][4 * g] * rd, acc[e % 3][4 * g + 1] * rd), pk(acc[e % 3][4 * g + 2] * rd, acc[e % 3][4 * g + 3] * rd)};
                    *(LAS u32x2*)(size_t)((stw ^ (16 * (4 * dt + g))) + tile * 8192) = o;
                }
                constexpr int o0 = (s >= 76) ? s - 76 : s - 44, otile = (s >= 76) ? 1 : 0;
                if constexpr ((s >= 44 && s < 44 + 18) || s >= 76) {
                    if constexpr ((o0 & 1) == 0 && o0 < 16) {
                        constexpr int i = o0 >> 1;
                        ob4[i & 1] = lds128((str ^ (64 * (i & 3))) + i * 1024 + otile * 8192);
                    }
                    if constexpr ((o0 & 1) == 1 && o0 >= 1 && o0 < 17) {
                        constexpr int i = o0 >> 1;
                        GAS u32x4* dst = (GAS u32x4*)(ob + otile * 8192 + i * 1024 + lane * 16);
                        *dst = ob4[i & 1];
                    }
                }
                sbar();
                if constexpr (s == 60) STAMP(9);
            });
            STAMP(10);
        }
        STAMP_OUT();
    } else {
        const int kw = wave;
        char* wh = ws + (size_t)head * W_HEAD;
        const __amdgpu_buffer_rsrc_t rs = __builtin_amdgcn_make_buffer_rsrc(wh, 0, W_HEAD, 0x00020000);
        if (threadIdx.x < 32) *(volatile LAS unsigned*)(size_t)(sb + L_MISC + 4 * threadIdx.x) = 0u;
        const u64 gen = __hip_atomic_load((GAS u64*)(wh + W_GEN), __ATOMIC_RELAXED, __HIP_MEMORY_SCOPE_AGENT);
        const int rhalf = kw >> 1, ra = 2 * c + (kw & 1);
        {
            const u32x4 pz = {POISON, POISON, POISON, POISON};
            __builtin_amdgcn_raw_buffer_store_b128(pz, rs, W_R + (rhalf * 16 + ra) * 1024 + lane * 16, 0, 16);
        }
        wgbar();
        bf16x8 pf[8];
        {
            u32x4 raw[8];
#pragma unroll
            for (int ks = 0; ks < 8; ++ks) raw[ks] = lds128(fa[ks] + L_P + kw * 8192);
#pragma unroll
            for (int ks = 0; ks < 8; ++ks) {
                u32x4 sc;
#pragma unroll
                for (int j = 0; j < 4; ++j) sc[j] = pk(bflo(raw[ks][j]) * C1, bfhi(raw[ks][j]) * C1);
                lds128w(fa[ks] + L_P + kw * 8192, sc);
                pf[ks] = asbf(sc);
            }
        }
        const u32x4 onesv = {h == 0 ? 0x3F803F80u : 0u, h == 0 ? 0x00003F80u : 0u, 0u, 0u};
        const bf16x8 bones = asbf(onesv);
        const u64 tag = mix64(gen);
        STAMP(1);
        const int tq = (lane & 15) >> 2, tp = lane & 3, tcb = (lane >> 4) & 1;
        const unsigned va0 = opaque(sb + L_V + (4 * h + tq) * 128 + 16 * (4 * (tq >> 1) + 2 * tcb + (tp >> 1)) + 8 * (tp & 1));
        const unsigned va1 = opaque(va0 ^ 64);
        auto vfrag = [&](unsigned base, int off) {
            const s16x4 lo = __builtin_amdgcn_ds_read_tr16_b64_v4i16((LAS s16x4*)(size_t)(base + off));
            const s16x4 hi = __builtin_amdgcn_ds_read_tr16_b64_v4i16((LAS s16x4*)(size_t)(base + off + 8 * 128));
            return __builtin_bit_cast(bf16x8, __builtin_shufflevector(lo, hi, 0, 1, 2, 3, 4, 5, 6, 7));
        };
        const u32x4 allones = {0x3F803F80u, 0x3F803F80u, 0x3F803F80u, 0x3F803F80u};

        unsigned phi[8][8];
        f32x16 x[3];
        const unsigned kna = opaque(sb + L_KN + r * 8);
        auto expair = [&](auto tt, auto kk) {
            constexpr int t = tt.v, k = kk.v;
            phi[t][k] = pk(ex2(x[t % 3][2 * k]), ex2(x[t % 3][2 * k + 1]));
            asm volatile("" : "+v"(phi[t][k]));
        };
        auto extterm = [&](auto tt, u32x2 en) {
            const u32x4 ext = {en.x, en.y, 0u, 0u};
            x[tt.v % 3] = mfma(asbf(ext), bones, x[tt.v % 3]);
        };
        sfor<0, 4>([&](auto jj) {
            constexpr int j = jj.v, t0 = 2 * j, t1 = 2 * j + 1;
            wgbar();
            u32x2 en0, en1;
            if constexpr (j > 0) { en0 = *(const LAS u32x2*)(size_t)(kna + (t0 - 2) * 256); en1 = *(const LAS u32x2*)(size_t)(kna + (t0 - 1) * 256); }
            u32x4 a0[8], a1[8];
#pragma unroll
            for (int ks = 0; ks < 8; ++ks) a0[ks] = lds128(fa[ks] + L_K + t0 * 8192);
            sbar();
            if constexpr (j > 0) { extterm(IC<t0 - 2>{}, en0); sbar(); extterm(IC<t0 - 1>{}, en1); sbar(); }
            sfor<0, 8>([&](auto kk) {
                constexpr int k = kk.v;
                a1[k] = lds128(fa[k] + L_K + t1 * 8192);
                x[t0 % 3] = mfma(asbf(a0[k]), pf[k], k == 0 ? f32x16{} : x[t0 % 3]);
                if constexpr (j > 0 && k > 0) expair(IC<t0 - 2>{}, IC<k - 1>{});
                sbar();
            });
            if constexpr (j > 0) { expair(IC<t0 - 2>{}, IC<7>{}); sbar(); }
            sfor<0, 8>([&](auto kk) {
                constexpr int k = kk.v;
                x[t1 % 3] = mfma(asbf(a1[k]), pf[k], k == 0 ? f32x16{} : x[t1 % 3]);
                if constexpr (j > 0) expair(IC<t0 - 1>{}, kk);
                sbar();
            });
            asm volatile("" : "+v"(x[t0 % 3]), "+v"(x[t1 % 3]));
            if constexpr (j == 0) { vmwait<0>(); }
            STAMP(12 + j);
        });
        STAMP(2);
        auto gemm2 = [&](auto t0, auto t1, auto half, f32x16 (&acc)[2], f32x16& sum) {
            constexpr int T0 = t0.v, N = t1.v - t0.v;
            constexpr bool first = half.v == 0 && T0 == 0;
            wgbar();
            u32x2 en0, en1;
            if constexpr (first) { en0 = *(const LAS u32x2*)(size_t)(kna + 6 * 256); en1 = *(const LAS u32x2*)(size_t)(kna + 7 * 256); }
            bf16x8 bv[N][2][2];
#pragma unroll
            for (int n = 0; n < N; ++n) {
                const int t = T0 + n, slot = half.v == 0 ? t : (t < 7 ? 8 + t : 0);
#pragma unroll
                for (int ks = 0; ks < 2; ++ks) { bv[n][ks][0] = vfrag(va0, slot * 4096 + ks * 2048); bv[n][ks][1] = vfrag(va1, slot * 4096 + ks * 2048); }
            }
            sbar();
            if constexpr (first) { extterm(IC<6>{}, en0); sbar(); extterm(IC<7>{}, en1); sbar(); }
            sfor<0, N * 2>([&](auto ss) {
                constexpr int n = ss.v >> 1, ks = ss.v & 1, t = T0 + n;
                const u32x4 a = {phi[t][4 * ks], phi[t][4 * ks + 1], phi[t][4 * ks + 2], phi[t][4 * ks + 3]};
                acc[0] = mfma(asbf(a), bv[n][ks][0], acc[0]);
                if constexpr (first) { expair(IC<6 + (ss.v >> 1)>{}, IC<(ss.v & 1) * 4>{}); sbar(); }
                acc[1] = mfma(asbf(a), bv[n][ks][1], acc[1]);
                if constexpr (first) { expair(IC<6 + (ss.v >> 1)>{}, IC<(ss.v & 1) * 4 + 1>{}); sbar(); }
                if constexpr (half.v == 0) sum = mfma(asbf(a), asbf(allones), sum);
                if constexpr (first) { expair(IC<6 + (ss.v >> 1)>{}, IC<(ss.v & 1) * 4 + 2>{}); expair(IC<6 + (ss.v >> 1)>{}, IC<(ss.v & 1) * 4 + 3>{}); sbar(); }
            });
            asm volatile("" : "+v"(acc[0]), "+v"(acc[1]));
            if constexpr (half.v == 0) asm volatile("" : "+v"(sum));
            sbar();
        };
        auto publish = [&](int half, f32x16 (&acc)[2]) {
#pragma unroll
            for (int dtl = 0; dtl < 2; ++dtl)
#pragma unroll
                for (int ks = 0; ks < 2; ++ks) {
                    const u32x4 v = {pk(acc[dtl][8 * ks], acc[dtl][8 * ks + 1]), pk(acc[dtl][8 * ks + 2], acc[dtl][8 * ks + 3]),
                                     pk(acc[dtl][8 * ks + 4], acc[dtl][8 * ks + 5]), pk(acc[dtl][8 * ks + 6], acc[dtl][8 * ks + 7])};
                    __builtin_amdgcn_raw_buffer_store_b128(v, rs, W_P1 + ((half * 16 + 4 * kw + 2 * dtl + ks) * 8 + c) * 1024 + lane * 16, 0, 16);
                }
        };
        {
            f32x16 acc[2] = {}, sum = {};
            gemm2(IC<0>{}, IC<2>{}, IC<0>{}, acc, sum);
            gemm2(IC<2>{}, IC<4>{}, IC<0>{}, acc, sum);
            gemm2(IC<4>{}, IC<6>{}, IC<0>{}, acc, sum);
            gemm2(IC<6>{}, IC<8>{}, IC<0>{}, acc, sum);
            publish(0, acc);
            if (r == 0) {
#pragma unroll
                for (int j = 0; j < 4; ++j) {
                    const u32x4 v = {__float_as_uint(sum[4 * j]), __float_as_uint(sum[4 * j + 1]), __float_as_uint(sum[4 * j + 2]), __float_as_uint(sum[4 * j + 3])};
                    __builtin_amdgcn_raw_buffer_store_b128(v, rs, W_KS + c * 512 + kw * 128 + h * 64 + j * 16, 0, 16);
                }
            }
        }
        STAMP(3);
        {
            f32x16 acc[2] = {}, dummy = {};
            gemm2(IC<0>{}, IC<2>{}, IC<1>{}, acc, dummy);
            gemm2(IC<2>{}, IC<4>{}, IC<1>{}, acc, dummy);
            gemm2(IC<4>{}, IC<6>{}, IC<1>{}, acc, dummy);
            gemm2(IC<6>{}, IC<7>{}, IC<1>{}, acc, dummy);
            gemm2(IC<7>{}, IC<8>{}, IC<1>{}, acc, dummy);
            if (lane == 0) prog_set(sb + L_KP + 4 * kw, KP_G2B);
            STAMP(4);
            publish(1, acc);
            vmwait<0>();
            if (lane == 0) __hip_atomic_store((GAS u64*)(wh + W_F1 + (kw * 8 + c) * 8), tag, __ATOMIC_RELAXED, __HIP_MEMORY_SCOPE_AGENT);
        }
        STAMP(5);
        {
            const unsigned fo = (((lane & 8) ? kw : (c >> 1)) * 8 + (lane & 7)) * 8;
            GAS u64* fp = (GAS u64*)(wh + W_F1 + fo);
            for (unsigned s = 0; s < SPIN_GLB; ++s) {
                const u64 v = __hip_atomic_load(fp, __ATOMIC_RELAXED, __HIP_MEMORY_SCOPE_AGENT);
                if (__all(v == tag)) break;
                __builtin_amdgcn_s_sleep(1);
            }
            cbar();
        }
        STAMP(6);
        if (c == 0 && kw == 0 && lane == 0) __hip_atomic_store((GAS u64*)(wh + W_GEN), tag, __ATOMIC_RELAXED, __HIP_MEMORY_SCOPE_AGENT);
        {
            u32x4 p[8];
#pragma unroll
            for (int i = 0; i < 8; ++i) p[i] = __builtin_amdgcn_raw_buffer_load_b128(rs, W_P1 + ((rhalf * 16 + ra) * 8 + i) * 1024 + lane * 16, 0, 16);
            const u32x4 ks4 = __builtin_amdgcn_raw_buffer_load_b128(rs, W_KS + (lane >> 3) * 512 + kw * 128 + (lane & 7) * 16, 0, 16);
            u32x4 sel;
            {
                const int i = (r & 3) + 4 * (r >> 3);
                const bool on = r < 16 && ((r >> 2) & 1) == h;
#pragma unroll
                for (int j = 0; j < 4; ++j) sel[j] = (on && (i >> 1) == j) ? ((i & 1) ? 0x3F800000u : 0x00003F80u) : 0u;
            }
            f32x16 d = {};
#pragma unroll
            for (int i = 0; i < 8; ++i) d = mfma(asbf(sel), asbf(p[i]), d);
            u32x4 o;
#pragma unroll
            for (int j = 0; j < 4; ++j) o[j] = min(pk(d[2 * j], d[2 * j + 1]), POISON - 1u);
            __builtin_amdgcn_raw_buffer_store_b128(o, rs, W_R + (rhalf * 16 + ra) * 1024 + lane * 16, 0, 16);
            STAMP(7);
            float s[4];
#pragma unroll
            for (int j = 0; j < 4; ++j) {
                float x = __uint_as_float(ks4[j]);
                x += __shfl_xor(x, 8); x += __shfl_xor(x, 16); x += __shfl_xor(x, 32);
                s[j] = x;
            }
            if (lane < 8) {
                const u32x2 kk = {pk(s[0], s[1]), pk(s[2], s[3])};
                *(LAS u32x2*)(size_t)(sb + L_KS + ((kw * 2 + ((lane >> 1) & 1)) * 2 + (lane >> 2)) * 16 + (lane & 1) * 8) = kk;
            }
            if (lane == 0) prog_set(sb + L_KP + 4 * kw, KP_KSUM);
        }
        {
            u32x4 g[8];
            for (unsigned s = 0; s < SPIN_GLB; ++s) {
                bool ok = true;
                cbar();
#pragma unroll
                for (int i = 0; i < 8; ++i) {
                    g[i] = __builtin_amdgcn_raw_buffer_load_b128(rs, W_R + ((i >> 2) * 16 + 4 * kw + (i & 3)) * 1024 + lane * 16, 0, 16);
                }
#pragma unroll
                for (int i = 0; i < 8; ++i) ok &= (g[i].x != POISON) & (g[i].y != POISON) & (g[i].z != POISON) & (g[i].w != POISON);
                if (__all(ok)) break;
                cbar();
            }
            STAMP(8);
            prog_wait(sb + L_KP, KP_G2B);
#pragma unroll
            for (int i = 0; i < 8; ++i) lds128w(sb + L_V + (i >> 2) * 16384 + (4 * kw + (i & 3)) * 1024 + lane * 16, g[i]);
            if (lane == 0) prog_set(sb + L_KP + 4 * kw, KP_KV);
        }
        STAMP(9);
        STAMP_OUT();
    }
}

extern "C" void kernel_launch(void* const* d_in, const int* in_sizes, int n_in, void* d_out, int out_size, void* d_ws,
                              size_t ws_size, hipStream_t stream) {
    performer_kernel<<<256, 512, 0, stream>>>((const char*)d_in[0], (const char*)d_in[1], (const char*)d_in[2],
                                              (const char*)d_in[3], (char*)d_out, (char*)d_ws);
}
```

```cpp
#include <hip/hip_runtime.h>
#include <hip/hip_bf16.h>
#include <stdint.h>

typedef __attribute__((ext_vector_type(8))) __bf16 bf16x8;
typedef __attribute__((ext_vector_type(2))) __bf16 bf16x2;
typedef __attribute__((ext_vector_type(4))) short s16x4;
typedef __attribute__((ext_vector_type(8))) short s16x8;
typedef __attribute__((ext_vector_type(16))) float f32x16;
typedef __attribute__((ext_vector_type(2))) float f32x2;
typedef __attribute__((ext_vector_type(4))) float f32x4;
typedef __attribute__((ext_vector_type(4))) unsigned u32x4;
typedef __attribute__((ext_vector_type(2))) unsigned u32x2;
typedef unsigned long long u64;
#define LAS __attribute__((address_space(3)))
#define GAS __attribute__((address_space(1)))
#define DI __device__ __forceinline__

#define LOAD_NT 1
#define STORE_MODE 18
#define WIN0 10
#define WIN1 8
#define QWIN 5
#define STAMPS 0
#define PROBE 0

constexpr int NH = 32, NS = 2048, ND = 128;
constexpr int ROWB = ND * 2;
constexpr unsigned L_K = 0;
constexpr unsigned L_V = 65536;
constexpr unsigned L_MISC = 65536 + 15 * 4096;
constexpr unsigned L_LQ = L_MISC;
constexpr unsigned L_KP = L_MISC + 64;
constexpr unsigned L_KS = L_MISC + 256;
constexpr unsigned L_KN = L_MISC + 1024;
constexpr unsigned L_P = 131072;
constexpr unsigned L_END = 163840;
constexpr unsigned W_GEN = 0;
constexpr unsigned W_F1 = 256;
constexpr unsigned W_KS = 1024;
constexpr unsigned W_R = 8192;
constexpr unsigned W_P1 = 8192 + 32768;
constexpr unsigned W_HEAD = W_P1 + 262144;
constexpr unsigned W_STAMP = NH * W_HEAD;

enum { KP_G2B = 1, KP_KSUM = 2, KP_KV = 3 };

constexpr float C1 = 0.29730177875068026f * 1.4426950408889634f;
constexpr float C2 = 0.5f * 0.08838834764831845f * 1.4426950408889634f;
constexpr float C3 = 3.5f;
constexpr unsigned SPIN_LDS = 1u << 20, SPIN_GLB = 1u << 16;
constexpr unsigned POISON = 0xFFFFFFFFu;

DI f32x16 mfma(bf16x8 a, bf16x8 b, f32x16 c) { return __builtin_amdgcn_mfma_f32_32x32x16_bf16(a, b, c, 0, 0, 0); }
DI unsigned pk(float a, float b) { f32x2 v = {a, b}; return __builtin_bit_cast(unsigned, __builtin_convertvector(v, bf16x2)); }
DI float dot2(unsigned a, unsigned b, float c) {
    return __builtin_amdgcn_fdot2_f32_bf16(__builtin_bit_cast(bf16x2, a), __builtin_bit_cast(bf16x2, b), c, false);
}
DI float bflo(unsigned x) { return __uint_as_float(x << 16); }
DI float bfhi(unsigned x) { return __uint_as_float(x & 0xFFFF0000u); }
DI bf16x8 asbf(u32x4 x) { return __builtin_bit_cast(bf16x8, x); }
DI u32x4 lds128(unsigned a) { return *(const LAS u32x4*)(size_t)a; }
DI void lds128w(unsigned a, u32x4 v) { *(LAS u32x4*)(size_t)a = v; }
DI void cbar() { asm volatile("" ::: "memory"); }
DI void wgbar() { asm volatile("s_waitcnt lgkmcnt(0)\n\ts_barrier" ::: "memory"); }
DI void sbar() { __builtin_amdgcn_sched_barrier(0); }
DI unsigned opaque(unsigned x) { asm volatile("" : "+v"(x)); return x; }
DI float ex2(float x) { return __builtin_amdgcn_exp2f(x); }
template <int CTRL> DI float dpp_add(float x) {
    return x + __uint_as_float(__builtin_amdgcn_update_dpp(0u, __float_as_uint(x), CTRL, 0xF, 0xF, true));
}
DI void pin8(unsigned (&x)[8]) {
    asm volatile("" : "+v"(x[0]), "+v"(x[1]), "+v"(x[2]), "+v"(x[3]), "+v"(x[4]), "+v"(x[5]), "+v"(x[6]), "+v"(x[7]));
}
template <int N> DI void vmwait() { asm volatile("s_waitcnt vmcnt(%0)" ::"n"(N) : "memory"); }

DI void glds(const char* sbase, unsigned voff, unsigned lds) {
    unsigned keep;
    asm volatile("s_mov_b32 %0, m0\n\ts_mov_b32 m0, %3\n\ts_nop 0\n\tglobal_load_lds_dwordx4 %1, %2 nt\n\ts_mov_b32 m0, %0"
                 : "=&s"(keep) : "v"(voff), "s"(sbase), "s"(lds) : "memory");
}

DI void prog_set(unsigned addr, unsigned v) { cbar(); *(volatile LAS unsigned*)(size_t)addr = v; cbar(); }
DI void prog_wait(unsigned addr4, unsigned need) {
    for (unsigned s = 0; s < SPIN_LDS; ++s) {
        const u32x4 v = *(volatile LAS u32x4*)(size_t)addr4;
        const unsigned m = min(min(v.x, v.y), min(v.z, v.w));
        if (__builtin_amdgcn_readfirstlane(m) >= need) break;
    }
    cbar();
}
DI u64 mix64(u64 x) {
    x += 0x9E3779B97F4A7C15ull; x = (x ^ (x >> 30)) * 0xBF58476D1CE4E5B9ull; x = (x ^ (x >> 27)) * 0x94D049BB133111EBull;
    return x ^ (x >> 31);
}
#define STAMP(i) do { } while (0)
#define STAMP_OUT() do { } while (0)

constexpr int I_K = 8, I_VA = 24, I_VB = 32, I_Q = 40, I_END = 56, QA_END = I_Q + 8;
constexpr int NGRP = 14;
constexpr int GRP[NGRP] = {8, 12, 16, 20, 24, 26, 28, 30, 32, 34, 36, 38, 39, 40};
constexpr int target(int c) {
    int t = c + (c < I_VA ? WIN0 : (c < I_VB + 2 ? WIN1 : QWIN));
    if (c < 30 && t > I_Q - 1) t = I_Q - 1;
    return t < I_END ? t : I_END;
}
constexpr int issued_at(int g) {
    int n = WIN0;
    for (int j = 0; j <= g; ++j) n = target(GRP[j]) > n ? target(GRP[j]) : n;
    return n;
}
constexpr int issued_q(int c) {
    int n = issued_at(NGRP - 1);
    for (int j = I_Q; j <= c; ++j) n = target(j) > n ? target(j) : n;
    return n;
}
constexpr int STOP = issued_q(QA_END);

template <int I> struct IC { static constexpr int v = I; };
template <int B, int E, class F> DI void sfor(F&& f) { if constexpr (B < E) { f(IC<B>{}); sfor<B + 1, E>(f); } }

__global__ __launch_bounds__(512, 2) void performer_kernel(const char* __restrict__ gq, const char* __restrict__ gk,
                                                           const char* __restrict__ gv, const char* __restrict__ gp,
                                                           char* go, char* ws) {
    __shared__ __attribute__((aligned(16))) char smem[L_END];
    const int lane = threadIdx.x & 63;
    const int wave = __builtin_amdgcn_readfirstlane(threadIdx.x >> 6);
    const int b = blockIdx.x, head = (b & 7) * 4 + (b >> 6), c = (b >> 3) & 7;
    const size_t rowoff = ((size_t)head * NS + c * 256) * ROWB;
    const unsigned sb = (unsigned)(size_t)(LAS char*)smem;
    const int r = lane & 31, h = lane >> 5;
    unsigned fa[8];
#pragma unroll
    for (int ks = 0; ks < 8; ++ks) fa[ks] = sb + r * 256 + 16 * ((2 * ks + h) ^ (r & 15));

    STAMP(0);
    if (wave >= 4) {
        const int qw = wave - 4;
        const char* kb = gk + rowoff; const char* vb = gv + rowoff; const char* qb = gq + rowoff + qw * 64 * ROWB;
        const char* pb = gp + qw * 32 * ROWB;
        unsigned qo[4];
#pragma unroll
        for (int i = 0; i < 4; ++i) { const int row = 4 * i + (lane >> 4); qo[i] = row * 256 + 16 * ((lane & 15) ^ (row & 15)); }
        unsigned ko[2];
#pragma unroll
        for (int i = 0; i < 2; ++i) { const int row = 8 * qw + 4 * i + (lane >> 4); ko[i] = row * 256 + 16 * ((lane & 15) ^ (row & 15)); }
        const int vrow = 8 * qw + (lane >> 3);
        const unsigned vo = vrow * 256 + 16 * ((lane & 7) ^ (((vrow >> 1) & 1) << 2));
        auto issue = [&](auto ii) {
            constexpr int i = ii.v;
            if constexpr (i < I_K) glds(pb + (i >> 2) * 4096, qo[i & 3], sb + L_P + qw * 8192 + i * 1024);
            else if constexpr (i < I_VA) { constexpr int t = (i - I_K) >> 1, j = (i - I_K) & 1; glds(kb + t * 8192, ko[j], sb + L_K + t * 8192 + (2 * qw + j) * 1024); }
            else if constexpr (i < I_VB) { constexpr int t = i - I_VA; glds(vb + t * 8192, vo, sb + L_V + t * 4096 + qw * 1024); }
            else if constexpr (i < I_Q) { constexpr int t = i - I_VB; glds(vb + t * 8192 + 128, vo, sb + L_V + (t < 7 ? 8 + t : 0) * 4096 + qw * 1024); }
            else { constexpr int j = i - I_Q; glds(qb + (j >> 2) * 4096, qo[j & 3], sb + L_K + (2 * qw) * 8192 + j * 1024); }
        };
        const unsigned nrd = sb + L_K + qw * 2048 + lane * 32;
        auto norms = [&](auto jj) {
            constexpr int j = jj.v;
            u32x4 x[2][2];
#pragma unroll
            for (int n = 0; n < 2; ++n) { x[n][0] = lds128(nrd + (2 * j + n) * 8192); x[n][1] = lds128(nrd + (2 * j + n) * 8192 + 16); }
            float s[2][2];
#pragma unroll
            for (int n = 0; n < 2; ++n) { s[n][0] = dot2(x[n][0].x, x[n][0].x, 0.f); s[n][1] = dot2(x[n][0].y, x[n][0].y, 0.f); }
#pragma unroll
            for (int n = 0; n < 2; ++n) { s[n][0] = dot2(x[n][0].z, x[n][0].z, s[n][0]); s[n][1] = dot2(x[n][0].w, x[n][0].w, s[n][1]); }
#pragma unroll
            for (int n = 0; n < 2; ++n) { s[n][0] = dot2(x[n][1].x, x[n][1].x, s[n][0]); s[n][1] = dot2(x[n][1].y, x[n][1].y, s[n][1]); }
#pragma unroll
            for (int n = 0; n < 2; ++n) { s[n][0] = dot2(x[n][1].z, x[n][1].z, s[n][0]); s[n][1] = dot2(x[n][1].w, x[n][1].w, s[n][1]); }
            float n0 = s[0][0] + s[0][1], n1 = s[1][0] + s[1][1];
            n0 = dpp_add<0xB1>(n0); n1 = dpp_add<0xB1>(n1);
            float n = ((lane ^ (lane >> 2)) & 1) ? n1 : n0;
            n = dpp_add<0x4E>(n); n = dpp_add<0x141>(n);
            const float e = -(C2 * n + C3);
            const unsigned p0 = pk(e, 0.f);
            const float e1 = e - bflo(p0);
            const unsigned p1 = pk(e1, 0.f);
            const float e2 = e1 - bflo(p1);
            const u32x2 ext = {(p0 & 0xFFFFu) | (p1 << 16), pk(e2, 0.f)};
            if ((lane & 6) == 0) *(LAS u32x2*)(size_t)(sb + L_KN + ((2 * j + (lane & 1)) * 32 + 8 * qw + (lane >> 3)) * 8) = ext;
        };
        sfor<0, WIN0>(issue);
        sfor<0, NGRP>([&](auto gg) {
            constexpr int g = gg.v, c = GRP[g], n0 = g == 0 ? WIN0 : issued_at(g - 1);
            vmwait<n0 - c>();
            sfor<n0, issued_at(g)>(issue);
            wgbar();
            if constexpr (g >= 1 && g <= 4) norms(IC<g - 1>{});
            if constexpr (g == 0) STAMP(11);
            if constexpr (g >= 1 && g <= 4) STAMP(11 + g);
            if constexpr (g == 4) STAMP(1);
            if constexpr (g == 8) STAMP(2);
            if constexpr (g == NGRP - 1) STAMP(3);
        });
        sfor<I_Q + 1, QA_END + 1>([&](auto cc) {
            constexpr int c = cc.v, n0 = issued_q(c - 1);
            vmwait<n0 - c>();
            sfor<n0, issued_q(c)>(issue);
        });
        STAMP(4);

        unsigned phq[2][4][8];
        unsigned fp[8];
#pragma unroll
        for (int ks = 0; ks < 8; ++ks) fp[ks] = opaque(fa[ks] + L_P);
        auto gemm3 = [&](auto tile) {
            u32x4 qf[8];
#pragma unroll
            for (int ks = 0; ks < 8; ++ks) qf[ks] = lds128(fa[ks] + L_K + (2 * qw + tile.v) * 8192);
            u32x4 pa[4];
#pragma unroll
            for (int g = 0; g < 3; ++g) pa[g] = lds128(fp[g]);
            float n0 = 0.f, n1 = 0.f;
#pragma unroll
            for (int ks = 0; ks < 8; ++ks) {
                n0 = dot2(qf[ks].x, qf[ks].x, n0); n1 = dot2(qf[ks].y, qf[ks].y, n1);
                n0 = dot2(qf[ks].z, qf[ks].z, n0); n1 = dot2(qf[ks].w, qf[ks].w, n1);
            }
            const float n = n0 + n1;
            const auto sw = __builtin_amdgcn_permlane32_swap(__float_as_uint(n), __float_as_uint(n), false, false);
            const float negn = -(C2 * (__uint_as_float(sw[0]) + __uint_as_float(sw[1])) + C3);
            f32x16 ci;
#pragma unroll
            for (int i = 0; i < 16; ++i) ci[i] = negn;
            f32x16 x[3];
            sbar();
            sfor<0, 42>([&](auto ss) {
                constexpr int s = ss.v;
                if constexpr (s + 3 < 32) pa[(s + 3) & 3] = lds128(fp[(s + 3) & 7] + ((s + 3) >> 3) * 8192);
                if constexpr (s < 32) {
                    constexpr int mt = s >> 3, ks = s & 7;
                    x[mt % 3] = mfma(asbf(pa[s & 3]), asbf(qf[ks]), ks == 0 ? ci : x[mt % 3]);
                }
                if constexpr (s >= 10) {
                    constexpr int mt = (s - 10) >> 3, k = (s - 10) & 7;
                    phq[tile.v][mt][k] = pk(ex2(x[mt % 3][2 * k]), ex2(x[mt % 3][2 * k + 1]));
                    asm volatile("" : "+v"(phq[tile.v][mt][k]));
                }
                if constexpr (tile.v == 0 && (s & 3) == 1 && STOP + (s >> 2) < I_END) issue(IC<STOP + (s >> 2)>{});
                sbar();
            });
        };
        static_assert(I_END - STOP <= 11, "tile b's loads must fit into tile a's slots");
        gemm3(IC<0>{});
        STAMP(5);
        vmwait<0>();
        gemm3(IC<1>{});
        STAMP(6);
        prog_wait(sb + L_KP, KP_KSUM);
        float rden[2];
        {
            f32x16 d0 = {}, d1 = {};
#pragma unroll
            for (int mt = 0; mt < 4; ++mt)
#pragma unroll
                for (int ks = 0; ks < 2; ++ks) {
                    const bf16x8 a = asbf(lds128(sb + L_KS + ((mt * 2 + ks) * 2 + h) * 16));
                    const u32x4 b0 = {phq[0][mt][4 * ks], phq[0][mt][4 * ks + 1], phq[0][mt][4 * ks + 2], phq[0][mt][4 * ks + 3]};
                    const u32x4 b1 = {phq[1][mt][4 * ks], phq[1][mt][4 * ks + 1], phq[1][mt][4 * ks + 2], phq[1][mt][4 * ks + 3]};
                    d0 = mfma(a, asbf(b0), d0); d1 = mfma(a, asbf(b1), d1);
                }
            rden[0] = __builtin_amdgcn_rcpf(d0[0] + 1e-6f); rden[1] = __builtin_amdgcn_rcpf(d1[0] + 1e-6f);
            asm volatile("" : "+v"(rden[0]), "+v"(rden[1]));
        }
        STAMP(7);
        prog_wait(sb + L_KP, KP_KV);
        STAMP(8);
        char* ob = go + rowoff + qw * 64 * ROWB;
        const __amdgpu_buffer_rsrc_t ors = __builtin_amdgcn_make_buffer_rsrc(ob, 0, 64 * ROWB, 0x00020000);
        {
            const unsigned kvb = opaque(sb + L_V + lane * 16);
            const unsigned stw = opaque(sb + L_K + 2 * qw * 8192 + r * 256 + 16 * (r & 15) + 8 * h);
            const unsigned str = opaque(sb + L_K + 2 * qw * 8192 + (lane >> 4) * 256 + 16 * ((lane & 15) ^ (lane >> 4)));
            auto afrag = [&](int g) { const int dt = (g >> 3) & 3, mt = (g >> 1) & 3, ks = g & 1;
                                      return lds128(kvb + (dt >> 1) * 16384 + (4 * mt + 2 * (dt & 1) + ks) * 1024); };
            u32x4 pa[4];
#pragma unroll
            for (int g = 0; g < 3; ++g) pa[g] = afrag(g);
            f32x16 acc[3];
            u32x4 ob4[2];
            sbar();
            sfor<0, 64 + 10 + 18>([&](auto ss) {
                constexpr int s = ss.v;
                if constexpr (s + 3 < 64) pa[(s + 3) & 3] = afrag(s + 3);
                if constexpr (s < 64) {
                    constexpr int tile = s >> 5, e = s >> 3, mt = (s >> 1) & 3, ks = s & 1;
                    const u32x4 bq = {phq[tile][mt][4 * ks], phq[tile][mt][4 * ks + 1], phq[tile][mt][4 * ks + 2], phq[tile][mt][4 * ks + 3]};
                    if constexpr ((s & 7) == 0) acc[e % 3] = mfma(asbf(pa[s & 3]), asbf(bq), f32x16{});
                    else acc[e % 3] = mfma(asbf(pa[s & 3]), asbf(bq), acc[e % 3]);
                }
                if constexpr (s >= 10 && s < 74 && ((s - 10) & 1) == 0) {
                    constexpr int e = (s - 10) >> 3, g = ((s - 10) & 7) >> 1, tile = e >> 2, dt = e & 3;
                    const float rd = rden[tile];
                    const u32x2 o = {pk(acc[e % 3][4 * g] * rd, acc[e % 3][4 * g + 1] * rd), pk(acc[e % 3][4 * g + 2] * rd, acc[e % 3][4 * g + 3] * rd)};
                    *(LAS u32x2*)(size_t)((stw ^ (16 * (4 * dt + g))) + tile * 8192) = o;
                }
                constexpr int o0 = (s >= 76) ? s - 76 : s - 44, otile = (s >= 76) ? 1 : 0;
                if constexpr ((s >= 44 && s < 44 + 18) || s >= 76) {
                    if constexpr ((o0 & 1) == 0 && o0 < 16) {
                        constexpr int i = o0 >> 1;
                        ob4[i & 1] = lds128((str ^ (64 * (i & 3))) + i * 1024 + otile * 8192);
                    }
                    if constexpr ((o0 & 1) == 1 && o0 >= 1 && o0 < 17) {
                        constexpr int i = o0 >> 1;
                        GAS u32x4* dst = (GAS u32x4*)(ob + otile * 8192 + i * 1024 + lane * 16);
                        __builtin_amdgcn_raw_buffer_store_b128(ob4[i & 1], ors, otile * 8192 + i * 1024 + lane * 16, 0, STORE_MODE);
                    }
                }
                sbar();
                if constexpr (s == 60) STAMP(9);
            });
            STAMP(10);
        }
        STAMP_OUT();
    } else {
        const int kw = wave;
        char* wh = ws + (size_t)head * W_HEAD;
        const __amdgpu_buffer_rsrc_t rs = __builtin_amdgcn_make_buffer_rsrc(wh, 0, W_HEAD, 0x00020000);
        if (threadIdx.x < 32) *(volatile LAS unsigned*)(size_t)(sb + L_MISC + 4 * threadIdx.x) = 0u;
        const u64 gen = __hip_atomic_load((GAS u64*)(wh + W_GEN), __ATOMIC_RELAXED, __HIP_MEMORY_SCOPE_AGENT);
        const int rhalf = kw >> 1, ra = 2 * c + (kw & 1);
        {
            const u32x4 pz = {POISON, POISON, POISON, POISON};
            __builtin_amdgcn_raw_buffer_store_b128(pz, rs, W_R + (rhalf * 16 + ra) * 1024 + lane * 16, 0, 16);
        }
        wgbar();
        bf16x8 pf[8];
        {
            u32x4 raw[8];
#pragma unroll
            for (int ks = 0; ks < 8; ++ks) raw[ks] = lds128(fa[ks] + L_P + kw * 8192);
#pragma unroll
            for (int ks = 0; ks < 8; ++ks) {
                u32x4 sc;
#pragma unroll
                for (int j = 0; j < 4; ++j) sc[j] = pk(bflo(raw[ks][j]) * C1, bfhi(raw[ks][j]) * C1);
                lds128w(fa[ks] + L_P + kw * 8192, sc);
                pf[ks] = asbf(sc);
            }
        }
        const u32x4 onesv = {h == 0 ? 0x3F803F80u : 0u, h == 0 ? 0x00003F80u : 0u, 0u, 0u};
        const bf16x8 bones = asbf(onesv);
        const u64 tag = mix64(gen);
        STAMP(1);
        const int tq = (lane & 15) >> 2, tp = lane & 3, tcb = (lane >> 4) & 1;
        const unsigned va0 = opaque(sb + L_V + (4 * h + tq) * 128 + 16 * (4 * (tq >> 1) + 2 * tcb + (tp >> 1)) + 8 * (tp & 1));
        const unsigned va1 = opaque(va0 ^ 64);
        auto vfrag = [&](unsigned base, int off) {
            const s16x4 lo = __builtin_amdgcn_ds_read_tr16_b64_v4i16((LAS s16x4*)(size_t)(base + off));
            const s16x4 hi = __builtin_amdgcn_ds_read_tr16_b64_v4i16((LAS s16x4*)(size_t)(base + off + 8 * 128));
            return __builtin_bit_cast(bf16x8, __builtin_shufflevector(lo, hi, 0, 1, 2, 3, 4, 5, 6, 7));
        };
        const u32x4 allones = {0x3F803F80u, 0x3F803F80u, 0x3F803F80u, 0x3F803F80u};

        unsigned phi[8][8];
        f32x16 x[3];
        const unsigned kna = opaque(sb + L_KN + r * 8);
        auto expair = [&](auto tt, auto kk) {
            constexpr int t = tt.v, k = kk.v;
            phi[t][k] = pk(ex2(x[t % 3][2 * k]), ex2(x[t % 3][2 * k + 1]));
            asm volatile("" : "+v"(phi[t][k]));
        };
        auto extterm = [&](auto tt, u32x2 en) {
            const u32x4 ext = {en.x, en.y, 0u, 0u};
            x[tt.v % 3] = mfma(asbf(ext), bones, x[tt.v % 3]);
        };
        sfor<0, 4>([&](auto jj) {
            constexpr int j = jj.v, t0 = 2 * j, t1 = 2 * j + 1;
            wgbar();
            u32x2 en0, en1;
            if constexpr (j > 0) { en0 = *(const LAS u32x2*)(size_t)(kna + (t0 - 2) * 256); en1 = *(const LAS u32x2*)(size_t)(kna + (t0 - 1) * 256); }
            u32x4 a0[8], a1[8];
#pragma unroll
            for (int ks = 0; ks < 8; ++ks) a0[ks] = lds128(fa[ks] + L_K + t0 * 8192);
            sbar();
            if constexpr (j > 0) { extterm(IC<t0 - 2>{}, en0); sbar(); extterm(IC<t0 - 1>{}, en1); sbar(); }
            sfor<0, 8>([&](auto kk) {
                constexpr int k = kk.v;
                a1[k] = lds128(fa[k] + L_K + t1 * 8192);
                x[t0 % 3] = mfma(asbf(a0[k]), pf[k], k == 0 ? f32x16{} : x[t0 % 3]);
                if constexpr (j > 0 && k > 0) expair(IC<t0 - 2>{}, IC<k - 1>{});
                sbar();
            });
            if constexpr (j > 0) { expair(IC<t0 - 2>{}, IC<7>{}); sbar(); }
            sfor<0, 8>([&](auto kk) {
                constexpr int k = kk.v;
                x[t1 % 3] = mfma(asbf(a1[k]), pf[k], k == 0 ? f32x16{} : x[t1 % 3]);
                if constexpr (j > 0) expair(IC<t0 - 1>{}, kk);
                sbar();
            });
            asm volatile("" : "+v"(x[t0 % 3]), "+v"(x[t1 % 3]));
            if constexpr (j == 0) { vmwait<0>(); }
            STAMP(12 + j);
        });
        STAMP(2);
        auto gemm2 = [&](auto t0, auto t1, auto half, f32x16 (&acc)[2], f32x16& sum) {
            constexpr int T0 = t0.v, N = t1.v - t0.v;
            constexpr bool first = half.v == 0 && T0 == 0;
            wgbar();
            u32x2 en0, en1;
            if constexpr (first) { en0 = *(const LAS u32x2*)(size_t)(kna + 6 * 256); en1 = *(const LAS u32x2*)(size_t)(kna + 7 * 256); }
            bf16x8 bv[N][2][2];
#pragma unroll
            for (int n = 0; n < N; ++n) {
                const int t = T0 + n, slot = half.v == 0 ? t : (t < 7 ? 8 + t : 0);
#pragma unroll
                for (int ks = 0; ks < 2; ++ks) { bv[n][ks][0] = vfrag(va0, slot * 4096 + ks * 2048); bv[n][ks][1] = vfrag(va1, slot * 4096 + ks * 2048); }
            }
            sbar();
            if constexpr (first) { extterm(IC<6>{}, en0); sbar(); extterm(IC<7>{}, en1); sbar(); }
            sfor<0, N * 2>([&](auto ss) {
                constexpr int n = ss.v >> 1, ks = ss.v & 1, t = T0 + n;
                const u32x4 a = {phi[t][4 * ks], phi[t][4 * ks + 1], phi[t][4 * ks + 2], phi[t][4 * ks + 3]};
                acc[0] = mfma(asbf(a), bv[n][ks][0], acc[0]);
                if constexpr (first) { expair(IC<6 + (ss.v >> 1)>{}, IC<(ss.v & 1) * 4>{}); sbar(); }
                acc[1] = mfma(asbf(a), bv[n][ks][1], acc[1]);
                if constexpr (first) { expair(IC<6 + (ss.v >> 1)>{}, IC<(ss.v & 1) * 4 + 1>{}); sbar(); }
                if constexpr (half.v == 0) sum = mfma(asbf(a), asbf(allones), sum);
                if constexpr (first) { expair(IC<6 + (ss.v >> 1)>{}, IC<(ss.v & 1) * 4 + 2>{}); expair(IC<6 + (ss.v >> 1)>{}, IC<(ss.v & 1) * 4 + 3>{}); sbar(); }
            });
            asm volatile("" : "+v"(acc[0]), "+v"(acc[1]));
            if constexpr (half.v == 0) asm volatile("" : "+v"(sum));
            sbar();
        };
        auto publish = [&](int half, f32x16 (&acc)[2]) {
#pragma unroll
            for (int dtl = 0; dtl < 2; ++dtl)
#pragma unroll
                for (int ks = 0; ks < 2; ++ks) {
                    const u32x4 v = {pk(acc[dtl][8 * ks], acc[dtl][8 * ks + 1]), pk(acc[dtl][8 * ks + 2], acc[dtl][8 * ks + 3]),
                                     pk(acc[dtl][8 * ks + 4], acc[dtl][8 * ks + 5]), pk(acc[dtl][8 * ks + 6], acc[dtl][8 * ks + 7])};
                    __builtin_amdgcn_raw_buffer_store_b128(v, rs, W_P1 + ((half * 16 + 4 * kw + 2 * dtl + ks) * 8 + c) * 1024 + lane * 16, 0, 16);
                }
        };
        {
            f32x16 acc[2] = {}, sum = {};
            gemm2(IC<0>{}, IC<2>{}, IC<0>{}, acc, sum);
            gemm2(IC<2>{}, IC<4>{}, IC<0>{}, acc, sum);
            gemm2(IC<4>{}, IC<6>{}, IC<0>{}, acc, sum);
            gemm2(IC<6>{}, IC<8>{}, IC<0>{}, acc, sum);
            publish(0, acc);
            if (r == 0) {
#pragma unroll
                for (int j = 0; j < 4; ++j) {
                    const u32x4 v = {__float_as_uint(sum[4 * j]), __float_as_uint(sum[4 * j + 1]), __float_as_uint(sum[4 * j + 2]), __float_as_uint(sum[4 * j + 3])};
                    __builtin_amdgcn_raw_buffer_store_b128(v, rs, W_KS + c * 512 + kw * 128 + h * 64 + j * 16, 0, 16);
                }
            }
        }
        STAMP(3);
        {
            f32x16 acc[2] = {}, dummy = {};
            gemm2(IC<0>{}, IC<2>{}, IC<1>{}, acc, dummy);
            gemm2(IC<2>{}, IC<4>{}, IC<1>{}, acc, dummy);
            gemm2(IC<4>{}, IC<6>{}, IC<1>{}, acc, dummy);
            gemm2(IC<6>{}, IC<7>{}, IC<1>{}, acc, dummy);
            gemm2(IC<7>{}, IC<8>{}, IC<1>{}, acc, dummy);
            if (lane == 0) prog_set(sb + L_KP + 4 * kw, KP_G2B);
            STAMP(4);
            publish(1, acc);
            vmwait<0>();
            if (lane == 0) __hip_atomic_store((GAS u64*)(wh + W_F1 + (kw * 8 + c) * 8), tag, __ATOMIC_RELAXED, __HIP_MEMORY_SCOPE_AGENT);
        }
        STAMP(5);
        {
            const unsigned fo = (((lane & 8) ? kw : (c >> 1)) * 8 + (lane & 7)) * 8;
            GAS u64* fp = (GAS u64*)(wh + W_F1 + fo);
            for (unsigned s = 0; s < SPIN_GLB; ++s) {
                const u64 v = __hip_atomic_load(fp, __ATOMIC_RELAXED, __HIP_MEMORY_SCOPE_AGENT);
                if (__all(v == tag)) break;
                __builtin_amdgcn_s_sleep(1);
            }
            cbar();
        }
        STAMP(6);
        if (c == 0 && kw == 0 && lane == 0) __hip_atomic_store((GAS u64*)(wh + W_GEN), tag, __ATOMIC_RELAXED, __HIP_MEMORY_SCOPE_AGENT);
        {
            u32x4 p[8];
#pragma unroll
            for (int i = 0; i < 8; ++i) p[i] = __builtin_amdgcn_raw_buffer_load_b128(rs, W_P1 + ((rhalf * 16 + ra) * 8 + i) * 1024 + lane * 16, 0, 16);
            const u32x4 ks4 = __builtin_amdgcn_raw_buffer_load_b128(rs, W_KS + (lane >> 3) * 512 + kw * 128 + (lane & 7) * 16, 0, 16);
            u32x4 sel;
            {
                const int i = (r & 3) + 4 * (r >> 3);
                const bool on = r < 16 && ((r >> 2) & 1) == h;
#pragma unroll
                for (int j = 0; j < 4; ++j) sel[j] = (on && (i >> 1) == j) ? ((i & 1) ? 0x3F800000u : 0x00003F80u) : 0u;
            }
            f32x16 d = {};
#pragma unroll
            for (int i = 0; i < 8; ++i) d = mfma(asbf(sel), asbf(p[i]), d);
            u32x4 o;
#pragma unroll
            for (int j = 0; j < 4; ++j) o[j] = min(pk(d[2 * j], d[2 * j + 1]), POISON - 1u);
            __builtin_amdgcn_raw_buffer_store_b128(o, rs, W_R + (rhalf * 16 + ra) * 1024 + lane * 16, 0, 16);
            STAMP(7);
            float s[4];
#pragma unroll
            for (int j = 0; j < 4; ++j) {
                float x = __uint_as_float(ks4[j]);
                x += __shfl_xor(x, 8); x += __shfl_xor(x, 16); x += __shfl_xor(x, 32);
                s[j] = x;
            }
            if (lane < 8) {
                const u32x2 kk = {pk(s[0], s[1]), pk(s[2], s[3])};
                *(LAS u32x2*)(size_t)(sb + L_KS + ((kw * 2 + ((lane >> 1) & 1)) * 2 + (lane >> 2)) * 16 + (lane & 1) * 8) = kk;
            }
            if (lane == 0) prog_set(sb + L_KP + 4 * kw, KP_KSUM);
        }
        {
            u32x4 g[8];
            for (unsigned s = 0; s < SPIN_GLB; ++s) {
                bool ok = true;
                cbar();
#pragma unroll
                for (int i = 0; i < 8; ++i) {
                    g[i] = __builtin_amdgcn_raw_buffer_load_b128(rs, W_R + ((i >> 2) * 16 + 4 * kw + (i & 3)) * 1024 + lane * 16, 0, 16);
                }
#pragma unroll
                for (int i = 0; i < 8; ++i) ok &= (g[i].x != POISON) & (g[i].y != POISON) & (g[i].z != POISON) & (g[i].w != POISON);
                if (__all(ok)) break;
                cbar();
            }
            STAMP(8);
            prog_wait(sb + L_KP, KP_G2B);
#pragma unroll
            for (int i = 0; i < 8; ++i) lds128w(sb + L_V + (i >> 2) * 16384 + (4 * kw + (i & 3)) * 1024 + lane * 16, g[i]);
            if (lane == 0) prog_set(sb + L_KP + 4 * kw, KP_KV);
        }
        STAMP(9);
        STAMP_OUT();
    }
}

extern "C" void kernel_launch(void* const* d_in, const int* in_sizes, int n_in, void* d_out, int out_size, void* d_ws,
                              size_t ws_size, hipStream_t stream) {
    performer_kernel<<<256, 512, 0, stream>>>((const char*)d_in[0], (const char*)d_in[1], (const char*)d_in[2],
                                              (const char*)d_in[3], (char*)d_out, (char*)d_ws);
}
```

```cpp
#include <hip/hip_runtime.h>
#include <hip/hip_bf16.h>
#include <stdint.h>

typedef __attribute__((ext_vector_type(8))) __bf16 bf16x8;
typedef __attribute__((ext_vector_type(2))) __bf16 bf16x2;
typedef __attribute__((ext_vector_type(4))) short s16x4;
typedef __attribute__((ext_vector_type(16))) float f32x16;
typedef __attribute__((ext_vector_type(2))) float f32x2;
typedef __attribute__((ext_vector_type(4))) unsigned u32x4;
typedef __attribute__((ext_vector_type(2))) unsigned u32x2;
typedef unsigned long long u64;
#define LAS __attribute__((address_space(3)))
#define GAS __attribute__((address_space(1)))
#define DI __device__ __forceinline__

constexpr int NH = 32, NS = 2048, ND = 128;
constexpr int ROWB = ND * 2;
constexpr unsigned L_K = 0;
constexpr unsigned L_V = 65536;
constexpr unsigned L_KVB = L_V, L_KVA = L_V + 16384;
constexpr unsigned L_MISC = L_V + 15 * 4096;
constexpr unsigned L_LQ = L_MISC;
constexpr unsigned L_KP = L_MISC + 64;
constexpr unsigned L_KS = L_MISC + 256;
constexpr unsigned L_KN = L_MISC + 1024;
constexpr unsigned L_P = 131072;
constexpr unsigned L_END = 163840;
constexpr unsigned W_GEN = 0;
constexpr unsigned W_F1 = 256;
constexpr unsigned W_KS = 1024;
constexpr unsigned W_R = 8192;
constexpr unsigned W_P1 = W_R + 32768;
constexpr unsigned W_HEAD = W_P1 + 262144;
enum { KP_G2B = 1, KP_KSUM = 2, KP_KVA = 3, KP_KVB = 2 };

constexpr float C1 = 0.29730177875068026f * 1.4426950408889634f;
constexpr float C2 = 0.5f * 0.08838834764831845f * 1.4426950408889634f;
constexpr float C3 = 3.5f;
constexpr unsigned SPIN_LDS = 1u << 20, SPIN_GLB = 1u << 16;
constexpr unsigned POISON = 0xFFFFFFFFu;
constexpr int AUX_SC1 = 16;

DI f32x16 mfma(bf16x8 a, bf16x8 b, f32x16 c) { return __builtin_amdgcn_mfma_f32_32x32x16_bf16(a, b, c, 0, 0, 0); }
DI unsigned pk(float a, float b) { f32x2 v = {a, b}; return __builtin_bit_cast(unsigned, __builtin_convertvector(v, bf16x2)); }
DI float dot2(unsigned a, unsigned b, float c) {
    return __builtin_amdgcn_fdot2_f32_bf16(__builtin_bit_cast(bf16x2, a), __builtin_bit_cast(bf16x2, b), c, false);
}
DI float bflo(unsigned x) { return __uint_as_float(x << 16); }
DI float bfhi(unsigned x) { return __uint_as_float(x & 0xFFFF0000u); }
DI float ex2(float x) { return __builtin_amdgcn_exp2f(x); }
DI bf16x8 asbf(u32x4 x) { return __builtin_bit_cast(bf16x8, x); }
DI u32x4 lds128(unsigned a) { return *(const LAS u32x4*)(size_t)a; }
DI void lds128w(unsigned a, u32x4 v) { *(LAS u32x4*)(size_t)a = v; }
DI void cbar() { asm volatile("" ::: "memory"); }
DI void sbar() { __builtin_amdgcn_sched_barrier(0); }
DI void wgbar() { asm volatile("s_waitcnt lgkmcnt(0)\n\ts_barrier" ::: "memory"); }
DI unsigned opaque(unsigned x) { asm volatile("" : "+v"(x)); return x; }
template <int N> DI void vmwait() { asm volatile("s_waitcnt vmcnt(%0)" ::"n"(N) : "memory"); }
template <int CTRL> DI float dpp_add(float x) {
    return x + __uint_as_float(__builtin_amdgcn_update_dpp(0u, __float_as_uint(x), CTRL, 0xF, 0xF, true));
}

template <bool NT> DI void glds(const char* sbase, unsigned voff, unsigned lds) {
    unsigned keep;
    if constexpr (NT)
        asm volatile("s_mov_b32 %0, m0\n\ts_mov_b32 m0, %3\n\ts_nop 0\n\tglobal_load_lds_dwordx4 %1, %2 nt\n\ts_mov_b32 m0, %0"
                     : "=&s"(keep) : "v"(voff), "s"(sbase), "s"(lds) : "memory");
    else
        asm volatile("s_mov_b32 %0, m0\n\ts_mov_b32 m0, %3\n\ts_nop 0\n\tglobal_load_lds_dwordx4 %1, %2\n\ts_mov_b32 m0, %0"
                     : "=&s"(keep) : "v"(voff), "s"(sbase), "s"(lds) : "memory");
}

DI void prog_set(unsigned addr, unsigned v) { cbar(); *(volatile LAS unsigned*)(size_t)addr = v; cbar(); }
DI void prog_wait4(unsigned addr, unsigned need) {
    for (unsigned s = 0; s < SPIN_LDS; ++s) {
        const u32x4 v = *(volatile LAS u32x4*)(size_t)addr;
        if (__builtin_amdgcn_readfirstlane(min(min(v.x, v.y), min(v.z, v.w))) >= need) break;
    }
    cbar();
}
DI void prog_wait2(unsigned addr, unsigned need) {
    for (unsigned s = 0; s < SPIN_LDS; ++s) {
        const u32x2 v = *(volatile LAS u32x2*)(size_t)addr;
        if (__builtin_amdgcn_readfirstlane(min(v.x, v.y)) >= need) break;
    }
    cbar();
}
DI u64 mix64(u64 x) {
    x += 0x9E3779B97F4A7C15ull; x = (x ^ (x >> 30)) * 0xBF58476D1CE4E5B9ull; x = (x ^ (x >> 27)) * 0x94D049BB133111EBull;
    return x ^ (x >> 31);
}

constexpr int I_K = 8, I_VA = 24, I_VB = 32, I_Q = 40, I_END = 56, QA_END = I_Q + 8;
constexpr int WIN_K = 10, WIN_V = 8, Q_START = 36;
constexpr int NGRP = 11;
constexpr int GRP[NGRP] = {8, 12, 16, 20, 24, 28, 32, 36, 38, 39, 40};
constexpr int grp_of(int c) { for (int g = 0; g < NGRP; ++g) if (GRP[g] == c) return g; return -1; }
constexpr int nland(int i) {
    int c = 0;
    for (int j = 0; j <= i; ++j) {
        int n = j - ((j < I_VA ? WIN_K : WIN_V) - 1);
        if (j == I_Q - 1 && n < I_VB) n = I_VB;
        if (j >= I_Q && n < Q_START) n = Q_START;
        c = n > c ? n : c;
    }
    return c;
}
static_assert(nland(I_Q - 1) >= I_VB && nland(I_END - 1) >= I_Q && nland(I_END - 1) <= QA_END && nland(I_K - 1) == 0, "load schedule");

template <int I> struct IC { static constexpr int v = I; };
template <int B, int E, class F> DI void sfor(F&& f) { if constexpr (B < E) { f(IC<B>{}); sfor<B + 1, E>(f); } }

__global__ __launch_bounds__(512, 2) void performer_kernel(const char* __restrict__ gq, const char* __restrict__ gk,
                                                           const char* __restrict__ gv, const char* __restrict__ gp, char* go, char* ws) {
    __shared__ __attribute__((aligned(16))) char smem[L_END];
    const int lane = threadIdx.x & 63;
    const int wave = __builtin_amdgcn_readfirstlane(threadIdx.x >> 6);
    const int b = blockIdx.x, head = (b & 7) * 4 + (b >> 6), c = (b >> 3) & 7;
    const size_t rowoff = ((size_t)head * NS + c * 256) * ROWB;
    const unsigned sb = (unsigned)(size_t)(LAS char*)smem;
    const int r = lane & 31, h = lane >> 5;
    unsigned fa[8];
#pragma unroll
    for (int ks = 0; ks < 8; ++ks) fa[ks] = sb + r * 256 + 16 * ((2 * ks + h) ^ (r & 15));

    if (wave >= 4) {
        const int qw = wave - 4;
        const char* kb = gk + rowoff; const char* vb = gv + rowoff; const char* qb = gq + rowoff + qw * 64 * ROWB;
        const char* pb = gp + qw * 32 * ROWB;
        unsigned qo[4];
#pragma unroll
        for (int i = 0; i < 4; ++i) { const int row = 4 * i + (lane >> 4); qo[i] = row * 256 + 16 * ((lane & 15) ^ (row & 15)); }
        unsigned ko[2];
#pragma unroll
        for (int i = 0; i < 2; ++i) { const int row = 8 * qw + 4 * i + (lane >> 4); ko[i] = row * 256 + 16 * ((lane & 15) ^ (row & 15)); }
        const int vrow = 8 * qw + (lane >> 3);
        const unsigned vo = vrow * 256 + 16 * ((lane & 7) ^ (((vrow >> 1) & 1) << 2));
        auto issue = [&](auto ii) {
            constexpr int i = ii.v;
            if constexpr (i < I_K) glds<false>(pb + (i >> 2) * 4096, qo[i & 3], sb + L_P + qw * 8192 + i * 1024);
            else if constexpr (i < I_VA) { constexpr int t = (i - I_K) >> 1, j = (i - I_K) & 1; glds<true>(kb + t * 8192, ko[j], sb + L_K + t * 8192 + (2 * qw + j) * 1024); }
            else if constexpr (i < I_VB) { constexpr int t = i - I_VA; glds<true>(vb + t * 8192, vo, sb + L_V + t * 4096 + qw * 1024); }
            else if constexpr (i < I_Q) { constexpr int t = i - I_VB; glds<true>(vb + t * 8192 + 128, vo, sb + L_V + (t < 7 ? 8 + t : 0) * 4096 + qw * 1024); }
            else { constexpr int j = i - I_Q; glds<true>(qb + (j >> 2) * 4096, qo[j & 3], sb + L_K + (2 * qw) * 8192 + j * 1024); }
        };
        const unsigned nrd = sb + L_K + qw * 2048 + lane * 32;
        auto norms = [&](auto jj) {
            constexpr int j = jj.v;
            u32x4 x[2][2];
#pragma unroll
            for (int n = 0; n < 2; ++n) { x[n][0] = lds128(nrd + (2 * j + n) * 8192); x[n][1] = lds128(nrd + (2 * j + n) * 8192 + 16); }
            float s[2][2];
#pragma unroll
            for (int n = 0; n < 2; ++n) { s[n][0] = dot2(x[n][0].x, x[n][0].x, 0.f); s[n][1] = dot2(x[n][0].y, x[n][0].y, 0.f); }
#pragma unroll
            for (int n = 0; n < 2; ++n) { s[n][0] = dot2(x[n][0].z, x[n][0].z, s[n][0]); s[n][1] = dot2(x[n][0].w, x[n][0].w, s[n][1]); }
#pragma unroll
            for (int n = 0; n < 2; ++n) { s[n][0] = dot2(x[n][1].x, x[n][1].x, s[n][0]); s[n][1] = dot2(x[n][1].y, x[n][1].y, s[n][1]); }
#pragma unroll
            for (int n = 0; n < 2; ++n) { s[n][0] = dot2(x[n][1].z, x[n][1].z, s[n][0]); s[n][1] = dot2(x[n][1].w, x[n][1].w, s[n][1]); }
            float n0 = s[0][0] + s[0][1], n1 = s[1][0] + s[1][1];
            n0 = dpp_add<0xB1>(n0); n1 = dpp_add<0xB1>(n1);
            float n = ((lane ^ (lane >> 2)) & 1) ? n1 : n0;
            n = dpp_add<0x4E>(n); n = dpp_add<0x141>(n);
            const float e = -(C2 * n + C3);
            const unsigned p0 = pk(e, 0.f);
            const float e1 = e - bflo(p0);
            const unsigned p1 = pk(e1, 0.f);
            const float e2 = e1 - bflo(p1);
            const u32x2 ext = {(p0 & 0xFFFFu) | (p1 << 16), pk(e2, 0.f)};
            if ((lane & 6) == 0) *(LAS u32x2*)(size_t)(sb + L_KN + ((2 * j + (lane & 1)) * 32 + 8 * qw + (lane >> 3)) * 8) = ext;
        };
        auto landed = [&](auto cc) {
            constexpr int g = grp_of(cc.v);
            if constexpr (g >= 0) {
                wgbar();
                if constexpr (g >= 1 && g <= 4) norms(IC<g - 1>{});
            }
        };
        sfor<0, I_END>([&](auto ii) {
            constexpr int i = ii.v, c0 = i > 0 ? nland(i - 1) : 0, c1 = nland(i);
            constexpr bool late = i == I_Q - 1 || c1 == I_K;
            if constexpr (c1 > c0) {
                sfor<c0 + 1, c1>([&](auto cc) { vmwait<i - cc.v>(); landed(cc); });
                vmwait<i - c1>();
            }
            if constexpr (c1 > c0 && late) landed(IC<c1>{});
            issue(ii);
            if constexpr (c1 > c0 && !late) landed(IC<c1>{});
        });
        vmwait<I_END - QA_END>();

        unsigned phq[2][4][8];
        unsigned fp[8];
#pragma unroll
        for (int ks = 0; ks < 8; ++ks) fp[ks] = opaque(fa[ks] + L_P);
        auto gemm3 = [&](auto tile) {
            u32x4 qf[8];
#pragma unroll
            for (int ks = 0; ks < 8; ++ks) qf[ks] = lds128(fa[ks] + L_K + (2 * qw + tile.v) * 8192);
            u32x4 pa[4];
#pragma unroll
            for (int g = 0; g < 3; ++g) pa[g] = lds128(fp[g]);
            float n0 = 0.f, n1 = 0.f;
#pragma unroll
            for (int ks = 0; ks < 8; ++ks) {
                n0 = dot2(qf[ks].x, qf[ks].x, n0); n1 = dot2(qf[ks].y, qf[ks].y, n1);
                n0 = dot2(qf[ks].z, qf[ks].z, n0); n1 = dot2(qf[ks].w, qf[ks].w, n1);
            }
            const float n = n0 + n1;
            const auto sw = __builtin_amdgcn_permlane32_swap(__float_as_uint(n), __float_as_uint(n), false, false);
            const float negn = -(C2 * (__uint_as_float(sw[0]) + __uint_as_float(sw[1])) + C3);
            f32x16 ci;
#pragma unroll
            for (int i = 0; i < 16; ++i) ci[i] = negn;
            f32x16 x[3];
            sbar();
            sfor<0, 42>([&](auto ss) {
                constexpr int s = ss.v;
                if constexpr (s + 3 < 32) pa[(s + 3) & 3] = lds128(fp[(s + 3) & 7] + ((s + 3) >> 3) * 8192);
                if constexpr (s < 32) {
                    constexpr int mt = s >> 3, ks = s & 7;
                    x[mt % 3] = mfma(asbf(pa[s & 3]), asbf(qf[ks]), ks == 0 ? ci : x[mt % 3]);
                }
                if constexpr (s >= 10) {
                    constexpr int mt = (s - 10) >> 3, k = (s - 10) & 7;
                    phq[tile.v][mt][k] = pk(ex2(x[mt % 3][2 * k]), ex2(x[mt % 3][2 * k + 1]));
                    asm volatile("" : "+v"(phq[tile.v][mt][k]));
                }
                sbar();
            });
        };
        gemm3(IC<0>{});
        vmwait<0>();
        if (lane == 0) prog_set(sb + L_LQ + 4 * qw, 1);
        gemm3(IC<1>{});
        prog_wait2(sb + L_KP, KP_KSUM);
        float rden[2];
        {
            f32x16 d0 = {}, d1 = {};
#pragma unroll
            for (int mt = 0; mt < 4; ++mt)
#pragma unroll
                for (int ks = 0; ks < 2; ++ks) {
                    const bf16x8 a = asbf(lds128(sb + L_KS + ((mt * 2 + ks) * 2 + h) * 16));
                    const u32x4 b0 = {phq[0][mt][4 * ks], phq[0][mt][4 * ks + 1], phq[0][mt][4 * ks + 2], phq[0][mt][4 * ks + 3]};
                    const u32x4 b1 = {phq[1][mt][4 * ks], phq[1][mt][4 * ks + 1], phq[1][mt][4 * ks + 2], phq[1][mt][4 * ks + 3]};
                    d0 = mfma(a, asbf(b0), d0); d1 = mfma(a, asbf(b1), d1);
                }
            rden[0] = __builtin_amdgcn_rcpf(d0[0] + 1e-6f); rden[1] = __builtin_amdgcn_rcpf(d1[0] + 1e-6f);
            asm volatile("" : "+v"(rden[0]), "+v"(rden[1]));
        }
        const __amdgpu_buffer_rsrc_t ors = __builtin_amdgcn_make_buffer_rsrc(go + rowoff + qw * 64 * ROWB, 0, 64 * ROWB, 0x00020000);
        const unsigned stw = opaque(sb + L_K + 2 * qw * 8192 + r * 128 + 16 * (r & 7) + 8 * h);
        const unsigned str = opaque(sb + L_K + 2 * qw * 8192 + (lane >> 3) * 128 + 16 * ((lane & 7) ^ (lane >> 3)));
        const unsigned oo = (lane >> 3) * 256 + (lane & 7) * 16;
        auto gemm4 = [&](auto hh) {
            constexpr int half = hh.v;
            const unsigned kvb = opaque(sb + (half ? L_KVB : L_KVA) + lane * 16);
            auto afrag = [&](int g) { const int dtl = (g >> 3) & 1, mt = (g >> 1) & 3, ks = g & 1; return lds128(kvb + (4 * mt + 2 * dtl + ks) * 1024); };
            u32x4 pa[4];
#pragma unroll
            for (int g = 0; g < 3; ++g) pa[g] = afrag(g);
            f32x16 acc[3];
            unsigned ow[2];
            u32x4 ob4[2];
            sbar();
            sfor<0, 51>([&](auto ss) {
                constexpr int s = ss.v;
                if constexpr (s + 3 < 32) pa[(s + 3) & 3] = afrag((s + 3) & 15);
                if constexpr (s < 32) {
                    constexpr int tile = s >> 4, e = s >> 3, mt = (s >> 1) & 3, ks = s & 1;
                    const u32x4 bq = {phq[tile][mt][4 * ks], phq[tile][mt][4 * ks + 1], phq[tile][mt][4 * ks + 2], phq[tile][mt][4 * ks + 3]};
                    if constexpr ((s & 7) == 0) acc[e % 3] = mfma(asbf(pa[s & 3]), asbf(bq), f32x16{});
                    else acc[e % 3] = mfma(asbf(pa[s & 3]), asbf(bq), acc[e % 3]);
                }
                if constexpr (s >= 10 && s < 42) {
                    constexpr int e = (s - 10) >> 3, k = (s - 10) & 7, tile = e >> 1, dtl = e & 1;
                    ow[k & 1] = pk(acc[e % 3][2 * k] * rden[tile], acc[e % 3][2 * k + 1] * rden[tile]);
                    if constexpr (k & 1) {
                        const u32x2 o = {ow[0], ow[1]};
                        *(LAS u32x2*)(size_t)((stw ^ (16 * (4 * dtl + (k >> 1)))) + (half * 2 + tile) * 4096) = o;
                    }
                }
                constexpr int otile = s >= 43 ? 1 : 0, o0 = s - (otile ? 43 : 27);
                if constexpr (o0 >= 0 && o0 < 8) {
                    if constexpr ((o0 & 1) == 0) ob4[(o0 >> 1) & 1] = lds128(str + (half * 2 + otile) * 4096 + (o0 >> 1) * 1024);
                    else __builtin_amdgcn_raw_buffer_store_b128(ob4[(o0 >> 1) & 1], ors, oo + (otile * 32 + 8 * (o0 >> 1)) * 256 + half * 128, 0, AUX_SC1);
                }
                sbar();
            });
        };
        prog_wait2(sb + L_KP, KP_KVA);
        gemm4(IC<0>{});
        prog_wait2(sb + L_KP + 8, KP_KVB);
        gemm4(IC<1>{});
    } else {
        const int kw = wave;
        __builtin_amdgcn_s_setprio(2);
        char* wh = ws + (size_t)head * W_HEAD;
        const __amdgpu_buffer_rsrc_t rs = __builtin_amdgcn_make_buffer_rsrc(wh, 0, W_HEAD, 0x00020000);
        if (threadIdx.x < 32) *(volatile LAS unsigned*)(size_t)(sb + L_MISC + 4 * threadIdx.x) = 0u;
        const u64 gen = __hip_atomic_load((GAS u64*)(wh + W_GEN), __ATOMIC_RELAXED, __HIP_MEMORY_SCOPE_AGENT);
        const int rhalf = kw >> 1, ra = 2 * c + (kw & 1);
        {
            const u32x4 pz = {POISON, POISON, POISON, POISON};
            __builtin_amdgcn_raw_buffer_store_b128(pz, rs, W_R + (rhalf * 16 + ra) * 1024 + lane * 16, 0, AUX_SC1);
        }
        wgbar();
        bf16x8 pf[8];
        {
            u32x4 raw[8];
#pragma unroll
            for (int ks = 0; ks < 8; ++ks) raw[ks] = lds128(fa[ks] + L_P + kw * 8192);
#pragma unroll
            for (int ks = 0; ks < 8; ++ks) {
                u32x4 sc;
#pragma unroll
                for (int j = 0; j < 4; ++j) sc[j] = pk(bflo(raw[ks][j]) * C1, bfhi(raw[ks][j]) * C1);
                lds128w(fa[ks] + L_P + kw * 8192, sc);
                pf[ks] = asbf(sc);
            }
        }
        const u32x4 onesv = {h == 0 ? 0x3F803F80u : 0u, h == 0 ? 0x00003F80u : 0u, 0u, 0u};
        const bf16x8 bones = asbf(onesv);
        const u64 tag = mix64(gen);
        const int tq = (lane & 15) >> 2, tp = lane & 3, tcb = (lane >> 4) & 1;
        const unsigned va0 = opaque(sb + L_V + (4 * h + tq) * 128 + 16 * (4 * (tq >> 1) + 2 * tcb + (tp >> 1)) + 8 * (tp & 1));
        const unsigned va1 = opaque(va0 ^ 64);
        auto vfrag = [&](unsigned base, int off) {
            const s16x4 lo = __builtin_amdgcn_ds_read_tr16_b64_v4i16((LAS s16x4*)(size_t)(base + off));
            const s16x4 hi = __builtin_amdgcn_ds_read_tr16_b64_v4i16((LAS s16x4*)(size_t)(base + off + 8 * 128));
            return __builtin_bit_cast(bf16x8, __builtin_shufflevector(lo, hi, 0, 1, 2, 3, 4, 5, 6, 7));
        };

        unsigned phi[8][8];
        f32x16 x[3];
        const unsigned kna = opaque(sb + L_KN + r * 8);
        float ksum0 = 0.f, ksum1 = 0.f;
        auto expair = [&](auto tt, auto kk) {
            constexpr int t = tt.v, k = kk.v;
            const float e0 = ex2(x[t % 3][2 * k]), e1 = ex2(x[t % 3][2 * k + 1]);
            phi[t][k] = pk(e0, e1);
            ksum0 += e0; ksum1 += e1;
            asm volatile("" : "+v"(phi[t][k]), "+v"(ksum0), "+v"(ksum1));
        };
        auto extterm = [&](auto tt, u32x2 en) {
            const u32x4 ext = {en.x, en.y, 0u, 0u};
            x[tt.v % 3] = mfma(asbf(ext), bones, x[tt.v % 3]);
        };
        sfor<0, 4>([&](auto jj) {
            constexpr int j = jj.v, t0 = 2 * j, t1 = 2 * j + 1;
            wgbar();
            u32x2 en0, en1;
            if constexpr (j > 0) { en0 = *(const LAS u32x2*)(size_t)(kna + (t0 - 2) * 256); en1 = *(const LAS u32x2*)(size_t)(kna + (t0 - 1) * 256); }
            u32x4 a0[8], a1[8];
#pragma unroll
            for (int ks = 0; ks < 8; ++ks) a0[ks] = lds128(fa[ks] + L_K + t0 * 8192);
            sbar();
            if constexpr (j > 0) { extterm(IC<t0 - 2>{}, en0); sbar(); extterm(IC<t0 - 1>{}, en1); sbar(); }
            sfor<0, 8>([&](auto kk) {
                constexpr int k = kk.v;
                a1[k] = lds128(fa[k] + L_K + t1 * 8192);
                x[t0 % 3] = mfma(asbf(a0[k]), pf[k], k == 0 ? f32x16{} : x[t0 % 3]);
                if constexpr (j > 0 && k > 0) expair(IC<t0 - 2>{}, IC<k - 1>{});
                sbar();
            });
            if constexpr (j > 0) { expair(IC<t0 - 2>{}, IC<7>{}); sbar(); }
            sfor<0, 8>([&](auto kk) {
                constexpr int k = kk.v;
                x[t1 % 3] = mfma(asbf(a1[k]), pf[k], k == 0 ? f32x16{} : x[t1 % 3]);
                if constexpr (j > 0) expair(IC<t0 - 1>{}, kk);
                sbar();
            });
            asm volatile("" : "+v"(x[t0 % 3]), "+v"(x[t1 % 3]));
            if constexpr (j == 0) vmwait<0>();
        });
        auto gemm2 = [&](auto t0, auto t1, auto half, f32x16 (&acc)[2]) {
            constexpr int T0 = t0.v, N = t1.v - t0.v;
            constexpr bool first = half.v == 0 && T0 == 0;
            wgbar();
            u32x2 en0, en1;
            if constexpr (first) { en0 = *(const LAS u32x2*)(size_t)(kna + 6 * 256); en1 = *(const LAS u32x2*)(size_t)(kna + 7 * 256); }
            bf16x8 bv[2][4];
            auto rd = [&](int n, int q) {
                const int t = T0 + n, slot = half.v == 0 ? t : (t < 7 ? 8 + t : 0);
                bv[n & 1][q] = vfrag((q & 1) ? va1 : va0, slot * 4096 + (q >> 1) * 2048);
            };
#pragma unroll
            for (int q = 0; q < 4; ++q) rd(0, q);
            sbar();
            if constexpr (first) { extterm(IC<6>{}, en0); sbar(); extterm(IC<7>{}, en1); sbar(); }
            sfor<0, N * 4>([&](auto ss) {
                constexpr int n = ss.v >> 2, q = ss.v & 3, t = T0 + n, ks = q >> 1;
                if constexpr (n + 1 < N) rd(n + 1, q);
                const u32x4 a = {phi[t][4 * ks], phi[t][4 * ks + 1], phi[t][4 * ks + 2], phi[t][4 * ks + 3]};
                acc[q & 1] = mfma(asbf(a), bv[n & 1][q], acc[q & 1]);
                if constexpr (first) expair(IC<6 + (ss.v >> 3)>{}, IC<(ss.v & 7)>{});
                sbar();
            });
            asm volatile("" : "+v"(acc[0]), "+v"(acc[1]));
            sbar();
        };
        auto publish = [&](int half, f32x16 (&acc)[2]) {
#pragma unroll
            for (int dtl = 0; dtl < 2; ++dtl)
#pragma unroll
                for (int ks = 0; ks < 2; ++ks) {
                    const u32x4 v = {pk(acc[dtl][8 * ks], acc[dtl][8 * ks + 1]), pk(acc[dtl][8 * ks + 2], acc[dtl][8 * ks + 3]),
                                     pk(acc[dtl][8 * ks + 4], acc[dtl][8 * ks + 5]), pk(acc[dtl][8 * ks + 6], acc[dtl][8 * ks + 7])};
                    __builtin_amdgcn_raw_buffer_store_b128(v, rs, W_P1 + ((half * 16 + 4 * kw + 2 * dtl + ks) * 8 + c) * 1024 + lane * 16, 0, AUX_SC1);
                }
        };
        auto flag = [&](int half) {
            if (lane == 0) __hip_atomic_store((GAS u64*)(wh + W_F1 + ((4 * half + kw) * 8 + c) * 8), tag, __ATOMIC_RELAXED, __HIP_MEMORY_SCOPE_AGENT);
        };
        {
            f32x16 acca[2] = {}, accb[2] = {};
            gemm2(IC<0>{}, IC<4>{}, IC<0>{}, acca);
            gemm2(IC<4>{}, IC<8>{}, IC<0>{}, acca);
            publish(0, acca);
            {
                const float s = ksum0 + ksum1;
                const auto sw = __builtin_amdgcn_permlane32_swap(__float_as_uint(s), __float_as_uint(s), false, false);
                const float tot = __uint_as_float(sw[0]) + __uint_as_float(sw[1]);
                if (lane < 32) __builtin_amdgcn_raw_buffer_store_b32(__float_as_uint(tot), rs, W_KS + c * 512 + kw * 128 + r * 4, 0, AUX_SC1);
            }
            gemm2(IC<0>{}, IC<4>{}, IC<1>{}, accb);
            gemm2(IC<4>{}, IC<6>{}, IC<1>{}, accb);
            vmwait<0>();
            flag(0);
            gemm2(IC<6>{}, IC<7>{}, IC<1>{}, accb);
            gemm2(IC<7>{}, IC<8>{}, IC<1>{}, accb);
            if (lane == 0) prog_set(sb + L_KP + 4 * kw, KP_G2B);
            const int fsel = lane >> 3;
            const unsigned fo = (rhalf * 4 + ((rhalf == 0 && fsel == 1) ? 2 * kw : (rhalf == 0 && fsel == 2) ? 2 * kw + 1 : (c >> 1))) * 64 + (lane & 7) * 8;
            GAS u64* fp = (GAS u64*)(wh + W_F1 + fo);
            publish(1, accb);
            sbar();
            bool seen = false;
            if (rhalf) {
                vmwait<0>();
                flag(1);
                prog_wait4(sb + L_LQ, 1);
            } else {
                u64 fv;
                asm volatile("global_load_dwordx2 %0, %1, off sc1" : "=v"(fv) : "v"(fp) : "memory");
                vmwait<1>();
                flag(1);
                asm volatile("s_waitcnt vmcnt(1)" : "+v"(fv) :: "memory");
                seen = __all(fv == tag);
            }
            cbar();
            if (!seen) {
                for (unsigned s = 0; s < SPIN_GLB; ++s) {
                    cbar();
                    if (__all(__hip_atomic_load(fp, __ATOMIC_RELAXED, __HIP_MEMORY_SCOPE_AGENT) == tag)) break;
                }
            }
            cbar();
        }
        {
            u32x4 p[8], ks4[2];
#pragma unroll
            for (int i = 0; i < 8; ++i) p[i] = __builtin_amdgcn_raw_buffer_load_b128(rs, W_P1 + ((rhalf * 16 + ra) * 8 + i) * 1024 + lane * 16, 0, AUX_SC1);
            if (rhalf == 0) {
#pragma unroll
                for (int n = 0; n < 2; ++n) ks4[n] = __builtin_amdgcn_raw_buffer_load_b128(rs, W_KS + (lane >> 3) * 512 + (2 * kw + n) * 128 + (lane & 7) * 16, 0, AUX_SC1);
            }
            sbar();
            if (c == 0 && kw == 0 && lane == 0) __hip_atomic_store((GAS u64*)(wh + W_GEN), tag, __ATOMIC_RELAXED, __HIP_MEMORY_SCOPE_AGENT);
            sbar();
            u32x4 sel;
            {
                const int i = (r & 3) + 4 * (r >> 3);
                const bool on = r < 16 && ((r >> 2) & 1) == h;
#pragma unroll
                for (int j = 0; j < 4; ++j) sel[j] = (on && (i >> 1) == j) ? ((i & 1) ? 0x3F800000u : 0x00003F80u) : 0u;
            }
            f32x16 d = {};
#pragma unroll
            for (int i = 0; i < 8; ++i) d = mfma(asbf(sel), asbf(p[i]), d);
            u32x4 o;
#pragma unroll
            for (int j = 0; j < 4; ++j) o[j] = min(pk(d[2 * j], d[2 * j + 1]), POISON - 1u);
            __builtin_amdgcn_raw_buffer_store_b128(o, rs, W_R + (rhalf * 16 + ra) * 1024 + lane * 16, 0, AUX_SC1);
            sbar();
            if (rhalf == 0) {
#pragma unroll
                for (int n = 0; n < 2; ++n) {
                    float s[4];
#pragma unroll
                    for (int j = 0; j < 4; ++j) {
                        float y = __uint_as_float(ks4[n][j]);
                        y += __shfl_xor(y, 8); y += __shfl_xor(y, 16); y += __shfl_xor(y, 32);
                        s[j] = y;
                    }
                    if (lane < 8) {
                        const u32x2 kk = {pk(s[0], s[1]), pk(s[2], s[3])};
                        *(LAS u32x2*)(size_t)(sb + L_KS + (((2 * kw + n) * 2 + (lane >> 2)) * 2 + (lane & 1)) * 16 + ((lane >> 1) & 1) * 8) = kk;
                    }
                }
                if (lane == 0) prog_set(sb + L_KP + 4 * kw, KP_KSUM);
            }
        }
        {
            GAS unsigned* sp = (GAS unsigned*)(wh + W_R + (rhalf * 16 + (kw & 1) * 8 + (lane & 7)) * 1024 + 1020);
            for (unsigned s = 0; s < SPIN_GLB; ++s) {
                cbar();
                if (__all(__hip_atomic_load(sp, __ATOMIC_RELAXED, __HIP_MEMORY_SCOPE_AGENT) != POISON)) break;
            }
            u32x4 g[8];
            const unsigned go8 = W_R + (rhalf * 16 + (kw & 1) * 8) * 1024 + lane * 16;
            for (unsigned s = 0; s < SPIN_GLB; ++s) {
                bool ok = true;
                cbar();
#pragma unroll
                for (int i = 0; i < 8; ++i) g[i] = __builtin_amdgcn_raw_buffer_load_b128(rs, go8 + i * 1024, 0, AUX_SC1);
                sbar();
#pragma unroll
                for (int i = 0; i < 8; ++i) ok &= (g[i].x != POISON) & (g[i].y != POISON) & (g[i].z != POISON) & (g[i].w != POISON);
                if (__all(ok)) break;
            }
            if (rhalf) prog_wait4(sb + L_KP, KP_G2B);
#pragma unroll
            for (int i = 0; i < 8; ++i) lds128w(sb + (rhalf ? L_KVB : L_KVA) + ((kw & 1) * 8 + i) * 1024 + lane * 16, g[i]);
            if (lane == 0) prog_set(sb + L_KP + 4 * kw, rhalf ? KP_KVB : KP_KVA);
        }
    }
}

extern "C" void kernel_launch(void* const* d_in, const int* in_sizes, int n_in, void* d_out, int out_size, void* d_ws,
                              size_t ws_size, hipStream_t stream) {
    performer_kernel<<<256, 512, 0, stream>>>((const char*)d_in[0], (const char*)d_in[1], (const char*)d_in[2],
                                              (const char*)d_in[3], (char*)d_out, (char*)d_ws);
}
```

```cpp
#include <hip/hip_runtime.h>
#include <hip/hip_bf16.h>
#include <stdint.h>

typedef __attribute__((ext_vector_type(8))) __bf16 bf16x8;
typedef __attribute__((ext_vector_type(2))) __bf16 bf16x2;
typedef __attribute__((ext_vector_type(4))) short s16x4;
typedef __attribute__((ext_vector_type(16))) float f32x16;
typedef __attribute__((ext_vector_type(2))) float f32x2;
typedef __attribute__((ext_vector_type(4))) unsigned u32x4;
typedef __attribute__((ext_vector_type(2))) unsigned u32x2;
typedef unsigned long long u64;
#define LAS __attribute__((address_space(3)))
#define GAS __attribute__((address_space(1)))
#define DI __device__ __forceinline__

constexpr int NH = 32, NS = 2048, ND = 128;
constexpr int ROWB = ND * 2;
constexpr unsigned L_K = 0;
constexpr unsigned L_V = 65536;
constexpr unsigned L_KVB = L_V, L_KVA = L_V + 16384;
constexpr unsigned L_OWNA = L_KVA, L_OWNB = L_KVB + 4096;
constexpr unsigned L_MISC = L_V + 15 * 4096;
constexpr unsigned L_LQ = L_MISC;
constexpr unsigned L_KP = L_MISC + 64;
constexpr unsigned L_OW = L_MISC + 96;
constexpr unsigned L_KS = L_MISC + 256;
constexpr unsigned L_KN = L_MISC + 1024;
constexpr unsigned L_P = 131072;
constexpr unsigned L_END = 163840;
constexpr unsigned W_GEN = 0;
constexpr unsigned W_F1 = 256;
constexpr unsigned W_KS = 1024;
constexpr unsigned W_R = 8192;
constexpr unsigned W_P1 = W_R + 32768;
constexpr unsigned W_HEAD = W_P1 + 262144;
enum { KP_G2B = 1, KP_KSUM = 2, KP_KVA = 3, KP_KVB = 2 };

constexpr float C1 = 0.29730177875068026f * 1.4426950408889634f;
constexpr float C2 = 0.5f * 0.08838834764831845f * 1.4426950408889634f;
constexpr float C3 = 3.5f;
constexpr unsigned SPIN_LDS = 1u << 20, SPIN_GLB = 1u << 16;
constexpr unsigned POISON = 0xFFFFFFFFu;
constexpr unsigned OOB = 0x7FFFF000u;
constexpr int AUX_SC1 = 16;
constexpr int CODE_PF = 21568;

DI f32x16 mfma(bf16x8 a, bf16x8 b, f32x16 c) { return __builtin_amdgcn_mfma_f32_32x32x16_bf16(a, b, c, 0, 0, 0); }
DI unsigned pk(float a, float b) { f32x2 v = {a, b}; return __builtin_bit_cast(unsigned, __builtin_convertvector(v, bf16x2)); }
DI float dot2(unsigned a, unsigned b, float c) {
    return __builtin_amdgcn_fdot2_f32_bf16(__builtin_bit_cast(bf16x2, a), __builtin_bit_cast(bf16x2, b), c, false);
}
DI float bflo(unsigned x) { return __uint_as_float(x << 16); }
DI float bfhi(unsigned x) { return __uint_as_float(x & 0xFFFF0000u); }
DI float ex2(float x) { return __builtin_amdgcn_exp2f(x); }
DI bf16x8 asbf(u32x4 x) { return __builtin_bit_cast(bf16x8, x); }
DI u32x4 lds128(unsigned a) { return *(const LAS u32x4*)(size_t)a; }
DI void lds128w(unsigned a, u32x4 v) { *(LAS u32x4*)(size_t)a = v; }
DI void cbar() { asm volatile("" ::: "memory"); }
DI void sbar() { __builtin_amdgcn_sched_barrier(0); }
DI void wgbar() { asm volatile("s_waitcnt lgkmcnt(0)\n\ts_barrier" ::: "memory"); }
DI unsigned opaque(unsigned x) { asm volatile("" : "+v"(x)); return x; }
template <int N> DI void vmwait() { asm volatile("s_waitcnt vmcnt(%0)" ::"n"(N) : "memory"); }
template <int CTRL> DI float dpp_add(float x) {
    return x + __uint_as_float(__builtin_amdgcn_update_dpp(0u, __float_as_uint(x), CTRL, 0xF, 0xF, true));
}

template <bool NT> DI void glds(const char* sbase, unsigned voff, unsigned lds) {
    unsigned keep;
    if constexpr (NT)
        asm volatile("s_mov_b32 %0, m0\n\ts_mov_b32 m0, %3\n\ts_nop 0\n\tglobal_load_lds_dwordx4 %1, %2 nt\n\ts_mov_b32 m0, %0"
                     : "=&s"(keep) : "v"(voff), "s"(sbase), "s"(lds) : "memory");
    else
        asm volatile("s_mov_b32 %0, m0\n\ts_mov_b32 m0, %3\n\ts_nop 0\n\tglobal_load_lds_dwordx4 %1, %2\n\ts_mov_b32 m0, %0"
                     : "=&s"(keep) : "v"(voff), "s"(sbase), "s"(lds) : "memory");
}

DI void prog_set(unsigned addr, unsigned v) { cbar(); *(volatile LAS unsigned*)(size_t)addr = v; cbar(); }
DI void prog_wait4(unsigned addr, unsigned need) {
    for (unsigned s = 0; s < SPIN_LDS; ++s) {
        const u32x4 v = *(volatile LAS u32x4*)(size_t)addr;
        if (__builtin_amdgcn_readfirstlane(min(min(v.x, v.y), min(v.z, v.w))) >= need) break;
    }
    cbar();
}
DI void prog_wait2(unsigned addr, unsigned need) {
    for (unsigned s = 0; s < SPIN_LDS; ++s) {
        const u32x2 v = *(volatile LAS u32x2*)(size_t)addr;
        if (__builtin_amdgcn_readfirstlane(min(v.x, v.y)) >= need) break;
    }
    cbar();
}
DI u64 mix64(u64 x) {
    x += 0x9E3779B97F4A7C15ull; x = (x ^ (x >> 30)) * 0xBF58476D1CE4E5B9ull; x = (x ^ (x >> 27)) * 0x94D049BB133111EBull;
    return x ^ (x >> 31);
}

constexpr int I_K = 8, I_VA = 24, I_VB = 32, I_Q = 40, I_END = 56, QA_END = I_Q + 8;
constexpr int WIN_K = 10, WIN_V = 8, Q_START = 36;
constexpr int NGRP = 11;
constexpr int GRP[NGRP] = {8, 12, 16, 20, 24, 28, 32, 36, 38, 39, 40};
constexpr int grp_of(int c) { for (int g = 0; g < NGRP; ++g) if (GRP[g] == c) return g; return -1; }
constexpr int nland(int i) {
    int c = 0;
    for (int j = 0; j <= i; ++j) {
        int n = j - ((j < I_VA ? WIN_K : WIN_V) - 1);
        if (j == I_Q - 1 && n < I_VB) n = I_VB;
        if (j >= I_Q && n < Q_START) n = Q_START;
        c = n > c ? n : c;
    }
    return c;
}
static_assert(nland(I_Q - 1) >= I_VB && nland(I_END - 1) >= I_Q && nland(I_END - 1) <= QA_END && nland(I_K - 1) == 0, "load schedule");

template <int I> struct IC { static constexpr int v = I; };
template <int B, int E, class F> DI void sfor(F&& f) { if constexpr (B < E) { f(IC<B>{}); sfor<B + 1, E>(f); } }

__global__ __launch_bounds__(512, 2) void performer_kernel(const char* __restrict__ gq, const char* __restrict__ gk,
                                                           const char* __restrict__ gv, const char* __restrict__ gp, char* go, char* ws) {
    __shared__ __attribute__((aligned(16))) char smem[L_END];
    unsigned long long pc0;
    asm volatile("s_getpc_b64 %0" : "=s"(pc0));
    const int lane = threadIdx.x & 63;
    const int wave = __builtin_amdgcn_readfirstlane(threadIdx.x >> 6);
    const int b = blockIdx.x, head = (b & 7) * 4 + (b >> 6), c = (b >> 3) & 7;
    const size_t rowoff = ((size_t)head * NS + c * 256) * ROWB;
    const unsigned sb = (unsigned)(size_t)(LAS char*)smem;
    const int r = lane & 31, h = lane >> 5;
    unsigned fa[8];
#pragma unroll
    for (int ks = 0; ks < 8; ++ks) fa[ks] = sb + r * 256 + 16 * ((2 * ks + h) ^ (r & 15));

    if (wave >= 4) {
        const int qw = wave - 4;
        const char* kb = gk + rowoff; const char* vb = gv + rowoff; const char* qb = gq + rowoff + qw * 64 * ROWB;
        const char* pb = gp + qw * 32 * ROWB;
        unsigned qo[4];
#pragma unroll
        for (int i = 0; i < 4; ++i) { const int row = 4 * i + (lane >> 4); qo[i] = row * 256 + 16 * ((lane & 15) ^ (row & 15)); }
        unsigned ko[2];
#pragma unroll
        for (int i = 0; i < 2; ++i) { const int row = 8 * qw + 4 * i + (lane >> 4); ko[i] = row * 256 + 16 * ((lane & 15) ^ (row & 15)); }
        const int vrow = 8 * qw + (lane >> 3);
        const unsigned vo = vrow * 256 + 16 * ((lane & 7) ^ (((vrow >> 1) & 1) << 2));
        auto issue = [&](auto ii) {
            constexpr int i = ii.v;
            if constexpr (i < I_K) glds<false>(pb + (i >> 2) * 4096, qo[i & 3], sb + L_P + qw * 8192 + i * 1024);
            else if constexpr (i < I_VA) { constexpr int t = (i - I_K) >> 1, j = (i - I_K) & 1; glds<true>(kb + t * 8192, ko[j], sb + L_K + t * 8192 + (2 * qw + j) * 1024); }
            else if constexpr (i < I_VB) { constexpr int t = i - I_VA; glds<true>(vb + t * 8192, vo, sb + L_V + t * 4096 + qw * 1024); }
            else if constexpr (i < I_Q) { constexpr int t = i - I_VB; glds<true>(vb + t * 8192 + 128, vo, sb + L_V + (t < 7 ? 8 + t : 0) * 4096 + qw * 1024); }
            else { constexpr int j = i - I_Q; glds<true>(qb + (j >> 2) * 4096, qo[j & 3], sb + L_K + (2 * qw) * 8192 + j * 1024); }
        };
        const unsigned nrd = sb + L_K + qw * 2048 + lane * 32;
        u32x4 nx[2][2];
        float n0, n1;
        u32x2 next;
        auto norms = [&](auto jj, auto st) {
            constexpr int j = jj.v, stage = st.v;
            if constexpr (stage == 0) {
#pragma unroll
                for (int n = 0; n < 2; ++n) { nx[n][0] = lds128(nrd + (2 * j + n) * 8192); nx[n][1] = lds128(nrd + (2 * j + n) * 8192 + 16); }
            } else if constexpr (stage == 1) {
                float s[2][2];
#pragma unroll
                for (int n = 0; n < 2; ++n) { s[n][0] = dot2(nx[n][0].x, nx[n][0].x, 0.f); s[n][1] = dot2(nx[n][0].y, nx[n][0].y, 0.f); }
#pragma unroll
                for (int n = 0; n < 2; ++n) { s[n][0] = dot2(nx[n][0].z, nx[n][0].z, s[n][0]); s[n][1] = dot2(nx[n][0].w, nx[n][0].w, s[n][1]); }
#pragma unroll
                for (int n = 0; n < 2; ++n) { s[n][0] = dot2(nx[n][1].x, nx[n][1].x, s[n][0]); s[n][1] = dot2(nx[n][1].y, nx[n][1].y, s[n][1]); }
#pragma unroll
                for (int n = 0; n < 2; ++n) { s[n][0] = dot2(nx[n][1].z, nx[n][1].z, s[n][0]); s[n][1] = dot2(nx[n][1].w, nx[n][1].w, s[n][1]); }
                n0 = s[0][0] + s[0][1]; n1 = s[1][0] + s[1][1];
                asm volatile("" : "+v"(n0), "+v"(n1));
            } else if constexpr (stage == 2) {
                n0 = dpp_add<0xB1>(n0); n1 = dpp_add<0xB1>(n1);
                float n = ((lane ^ (lane >> 2)) & 1) ? n1 : n0;
                n = dpp_add<0x4E>(n); n = dpp_add<0x141>(n);
                const float e = -(C2 * n + C3);
                const unsigned p0 = pk(e, 0.f);
                const float e1 = e - bflo(p0);
                const unsigned p1 = pk(e1, 0.f);
                const float e2 = e1 - bflo(p1);
                next = u32x2{(p0 & 0xFFFFu) | (p1 << 16), pk(e2, 0.f)};
                asm volatile("" : "+v"(next));
            } else {
                if ((lane & 6) == 0) *(LAS u32x2*)(size_t)(sb + L_KN + ((2 * j + (lane & 1)) * 32 + 8 * qw + (lane >> 3)) * 8) = next;
            }
        };
        auto landed = [&](auto cc) {
            constexpr int c_ = cc.v, g = grp_of(c_);
            if constexpr (g >= 0) wgbar();
            if constexpr (c_ >= 12 && c_ < 28) norms(IC<(c_ - 12) / 4>{}, IC<(c_ - 12) % 4>{});
        };
        sfor<0, I_END>([&](auto ii) {
            constexpr int i = ii.v, c0 = i > 0 ? nland(i - 1) : 0, c1 = nland(i);
            constexpr bool late = i == I_Q - 1 || c1 == I_K;
            if constexpr (c1 > c0) {
                sfor<c0 + 1, c1>([&](auto cc) { vmwait<i - cc.v>(); landed(cc); });
                vmwait<i - c1>();
            }
            if constexpr (c1 > c0 && late) landed(IC<c1>{});
            issue(ii);
            if constexpr (c1 > c0 && !late) landed(IC<c1>{});
        });
        vmwait<I_END - QA_END>();

        unsigned phq[2][4][8];
        unsigned fp[8];
#pragma unroll
        for (int ks = 0; ks < 8; ++ks) fp[ks] = opaque(fa[ks] + L_P);
        auto gemm3 = [&](auto tile) {
            u32x4 qf[8];
#pragma unroll
            for (int ks = 0; ks < 8; ++ks) qf[ks] = lds128(fa[ks] + L_K + (2 * qw + tile.v) * 8192);
            u32x4 pa[4];
#pragma unroll
            for (int g = 0; g < 3; ++g) pa[g] = lds128(fp[g]);
            float n0 = 0.f, n1 = 0.f;
#pragma unroll
            for (int ks = 0; ks < 8; ++ks) {
                n0 = dot2(qf[ks].x, qf[ks].x, n0); n1 = dot2(qf[ks].y, qf[ks].y, n1);
                n0 = dot2(qf[ks].z, qf[ks].z, n0); n1 = dot2(qf[ks].w, qf[ks].w, n1);
            }
            const float n = n0 + n1;
            const auto sw = __builtin_amdgcn_permlane32_swap(__float_as_uint(n), __float_as_uint(n), false, false);
            const float negn = -(C2 * (__uint_as_float(sw[0]) + __uint_as_float(sw[1])) + C3);
            f32x16 ci;
#pragma unroll
            for (int i = 0; i < 16; ++i) ci[i] = negn;
            f32x16 x[3];
            sbar();
            sfor<0, 42>([&](auto ss) {
                constexpr int s = ss.v;
                if constexpr (s + 3 < 32) pa[(s + 3) & 3] = lds128(fp[(s + 3) & 7] + ((s + 3) >> 3) * 8192);
                if constexpr (s < 32) {
                    constexpr int mt = s >> 3, ks = s & 7;
                    x[mt % 3] = mfma(asbf(pa[s & 3]), asbf(qf[ks]), ks == 0 ? ci : x[mt % 3]);
                }
                if constexpr (s >= 10) {
                    constexpr int mt = (s - 10) >> 3, k = (s - 10) & 7;
                    phq[tile.v][mt][k] = pk(ex2(x[mt % 3][2 * k]), ex2(x[mt % 3][2 * k + 1]));
                    asm volatile("" : "+v"(phq[tile.v][mt][k]));
                }
                sbar();
            });
        };
        gemm3(IC<0>{});
        vmwait<0>();
        if (lane == 0) prog_set(sb + L_LQ + 4 * qw, 1);
        gemm3(IC<1>{});
        prog_wait2(sb + L_KP, KP_KSUM);
        float rden[2];
        {
            f32x16 d0 = {}, d1 = {};
#pragma unroll
            for (int mt = 0; mt < 4; ++mt)
#pragma unroll
                for (int ks = 0; ks < 2; ++ks) {
                    const bf16x8 a = asbf(lds128(sb + L_KS + ((mt * 2 + ks) * 2 + h) * 16));
                    const u32x4 b0 = {phq[0][mt][4 * ks], phq[0][mt][4 * ks + 1], phq[0][mt][4 * ks + 2], phq[0][mt][4 * ks + 3]};
                    const u32x4 b1 = {phq[1][mt][4 * ks], phq[1][mt][4 * ks + 1], phq[1][mt][4 * ks + 2], phq[1][mt][4 * ks + 3]};
                    d0 = mfma(a, asbf(b0), d0); d1 = mfma(a, asbf(b1), d1);
                }
            rden[0] = __builtin_amdgcn_rcpf(d0[0] + 1e-6f); rden[1] = __builtin_amdgcn_rcpf(d1[0] + 1e-6f);
            asm volatile("" : "+v"(rden[0]), "+v"(rden[1]));
        }
        const __amdgpu_buffer_rsrc_t ors = __builtin_amdgcn_make_buffer_rsrc(go + rowoff + qw * 64 * ROWB, 0, 64 * ROWB, 0x00020000);
        const unsigned stw = opaque(sb + L_K + 2 * qw * 8192 + r * 128 + 16 * (r & 7) + 8 * h);
        const unsigned str = opaque(sb + L_K + 2 * qw * 8192 + (lane >> 3) * 128 + 16 * ((lane & 7) ^ (lane >> 3)));
        const unsigned oo = (lane >> 3) * 256 + (lane & 7) * 16;
        auto gemm4 = [&](auto hh) {
            constexpr int half = hh.v;
            const unsigned kvb = opaque(sb + (half ? L_KVB : L_KVA) + lane * 16);
            auto afrag = [&](int g) { const int dtl = (g >> 3) & 1, mt = (g >> 1) & 3, ks = g & 1; return lds128(kvb + (4 * mt + 2 * dtl + ks) * 1024); };
            u32x4 pa[4];
#pragma unroll
            for (int g = 0; g < 3; ++g) pa[g] = afrag(g);
            f32x16 acc[3];
            unsigned ow[2];
            u32x4 ob4[2];
            sbar();
            sfor<0, 51>([&](auto ss) {
                constexpr int s = ss.v;
                if constexpr (s + 3 < 32) pa[(s + 3) & 3] = afrag((s + 3) & 15);
                if constexpr (s < 32) {
                    constexpr int tile = s >> 4, e = s >> 3, mt = (s >> 1) & 3, ks = s & 1;
                    const u32x4 bq = {phq[tile][mt][4 * ks], phq[tile][mt][4 * ks + 1], phq[tile][mt][4 * ks + 2], phq[tile][mt][4 * ks + 3]};
                    if constexpr ((s & 7) == 0) acc[e % 3] = mfma(asbf(pa[s & 3]), asbf(bq), f32x16{});
                    else acc[e % 3] = mfma(asbf(pa[s & 3]), asbf(bq), acc[e % 3]);
                }
                if constexpr (s >= 10 && s < 42) {
                    constexpr int e = (s - 10) >> 3, k = (s - 10) & 7, tile = e >> 1, dtl = e & 1;
                    ow[k & 1] = pk(acc[e % 3][2 * k] * rden[tile], acc[e % 3][2 * k + 1] * rden[tile]);
                    if constexpr (k & 1) {
                        const u32x2 o = {ow[0], ow[1]};
                        *(LAS u32x2*)(size_t)((stw ^ (16 * (4 * dtl + (k >> 1)))) + (half * 2 + tile) * 4096) = o;
                    }
                }
                constexpr int otile = s >= 43 ? 1 : 0, o0 = s - (otile ? 43 : 27);
                if constexpr (o0 >= 0 && o0 < 8) {
                    if constexpr ((o0 & 1) == 0) ob4[(o0 >> 1) & 1] = lds128(str + (half * 2 + otile) * 4096 + (o0 >> 1) * 1024);
                    else __builtin_amdgcn_raw_buffer_store_b128(ob4[(o0 >> 1) & 1], ors, oo + (otile * 32 + 8 * (o0 >> 1)) * 256 + half * 128, 0, AUX_SC1);
                }
                sbar();
            });
        };
        prog_wait2(sb + L_KP, KP_KVA);
        gemm4(IC<0>{});
        prog_wait2(sb + L_KP + 8, KP_KVB);
        gemm4(IC<1>{});
    } else {
        const int kw = wave;
        __builtin_amdgcn_s_setprio(2);
        char* wh = ws + (size_t)head * W_HEAD;
        const __amdgpu_buffer_rsrc_t rs = __builtin_amdgcn_make_buffer_rsrc(wh, 0, W_HEAD, 0x00020000);
        if (threadIdx.x < 32) *(volatile LAS unsigned*)(size_t)(sb + L_MISC + 4 * threadIdx.x) = 0u;
        const u64 gen = __hip_atomic_load((GAS u64*)(wh + W_GEN), __ATOMIC_RELAXED, __HIP_MEMORY_SCOPE_AGENT);
        constexpr int NPF = (CODE_PF + 4095) / 4096;
        unsigned cpf[NPF] = {};
        if (kw == 3 && b < 8) {
#pragma unroll
            for (int i = 0; i < NPF; ++i) cpf[i] = *(const GAS unsigned*)((pc0 & ~63ull) + min(i * 4096 + lane * 64, CODE_PF - 64));
        }
        const int rhalf = kw >> 1, ra = 2 * c + (kw & 1);
        {
            const u32x4 pz = {POISON, POISON, POISON, POISON};
            __builtin_amdgcn_raw_buffer_store_b128(pz, rs, W_R + (rhalf * 16 + ra) * 1024 + lane * 16, 0, AUX_SC1);
        }
        wgbar();
        bf16x8 pf[8];
        u32x4 raw[8];
#pragma unroll
        for (int ks = 0; ks < 8; ++ks) raw[ks] = lds128(fa[ks] + L_P + kw * 8192);
        auto scale = [&](auto kk) {
            constexpr int ks = kk.v;
            u32x4 sc;
#pragma unroll
            for (int j = 0; j < 4; ++j) sc[j] = pk(bflo(raw[ks][j]) * C1, bfhi(raw[ks][j]) * C1);
            lds128w(fa[ks] + L_P + kw * 8192, sc);
            pf[ks] = asbf(sc);
            asm volatile("" : "+v"(pf[ks]));
        };
        sfor<0, 3>(scale);
        const u32x4 onesv = {h == 0 ? 0x3F803F80u : 0u, h == 0 ? 0x00003F80u : 0u, 0u, 0u};
        const bf16x8 bones = asbf(onesv);
        const u64 tag = mix64(gen);
        const int tq = (lane & 15) >> 2, tp = lane & 3, tcb = (lane >> 4) & 1;
        const unsigned va0 = opaque(sb + L_V + (4 * h + tq) * 128 + 16 * (4 * (tq >> 1) + 2 * tcb + (tp >> 1)) + 8 * (tp & 1));
        const unsigned va1 = opaque(va0 ^ 64);
        auto vfrag = [&](unsigned base, int off) {
            const s16x4 lo = __builtin_amdgcn_ds_read_tr16_b64_v4i16((LAS s16x4*)(size_t)(base + off));
            const s16x4 hi = __builtin_amdgcn_ds_read_tr16_b64_v4i16((LAS s16x4*)(size_t)(base + off + 8 * 128));
            return __builtin_bit_cast(bf16x8, __builtin_shufflevector(lo, hi, 0, 1, 2, 3, 4, 5, 6, 7));
        };

        unsigned phi[8][8];
        f32x16 x[3];
        const unsigned kna = opaque(sb + L_KN + r * 8);
        float ksum0 = 0.f, ksum1 = 0.f;
        auto expair = [&](auto tt, auto kk) {
            constexpr int t = tt.v, k = kk.v;
            const float e0 = ex2(x[t % 3][2 * k]), e1 = ex2(x[t % 3][2 * k + 1]);
            phi[t][k] = pk(e0, e1);
            ksum0 += e0; ksum1 += e1;
            asm volatile("" : "+v"(phi[t][k]), "+v"(ksum0), "+v"(ksum1));
        };
        auto extterm = [&](auto tt, u32x2 en) {
            const u32x4 ext = {en.x, en.y, 0u, 0u};
            x[tt.v % 3] = mfma(asbf(ext), bones, x[tt.v % 3]);
        };
        sfor<0, 4>([&](auto jj) {
            constexpr int j = jj.v, t0 = 2 * j, t1 = 2 * j + 1;
            wgbar();
            u32x2 en0, en1;
            if constexpr (j > 0) { en0 = *(const LAS u32x2*)(size_t)(kna + (t0 - 2) * 256); en1 = *(const LAS u32x2*)(size_t)(kna + (t0 - 1) * 256); }
            u32x4 a0[8], a1[8];
#pragma unroll
            for (int ks = 0; ks < 8; ++ks) a0[ks] = lds128(fa[ks] + L_K + t0 * 8192);
            sbar();
            if constexpr (j > 0) { extterm(IC<t0 - 2>{}, en0); sbar(); extterm(IC<t0 - 1>{}, en1); sbar(); }
            sfor<0, 8>([&](auto kk) {
                constexpr int k = kk.v;
                a1[k] = lds128(fa[k] + L_K + t1 * 8192);
                x[t0 % 3] = mfma(asbf(a0[k]), pf[k], k == 0 ? f32x16{} : x[t0 % 3]);
                if constexpr (j == 0 && k + 3 < 8) scale(IC<k + 3>{});
                if constexpr (j > 0 && k > 0) expair(IC<t0 - 2>{}, IC<k - 1>{});
                sbar();
            });
            if constexpr (j > 0) { expair(IC<t0 - 2>{}, IC<7>{}); sbar(); }
            sfor<0, 8>([&](auto kk) {
                constexpr int k = kk.v;
                x[t1 % 3] = mfma(asbf(a1[k]), pf[k], k == 0 ? f32x16{} : x[t1 % 3]);
                if constexpr (j > 0) expair(IC<t0 - 1>{}, kk);
                sbar();
            });
            asm volatile("" : "+v"(x[t0 % 3]), "+v"(x[t1 % 3]));
            if constexpr (j == 0) {
                vmwait<0>();
#pragma unroll
                for (int i = 0; i < NPF; ++i) { const unsigned t_ = cpf[i]; asm volatile("" :: "v"(t_)); }
            }
        });
        auto gemm2 = [&](auto t0, auto t1, auto half, f32x16 (&acc)[2]) {
            constexpr int T0 = t0.v, N = t1.v - t0.v;
            constexpr bool first = half.v == 0 && T0 == 0;
            wgbar();
            u32x2 en0, en1;
            if constexpr (first) { en0 = *(const LAS u32x2*)(size_t)(kna + 6 * 256); en1 = *(const LAS u32x2*)(size_t)(kna + 7 * 256); }
            bf16x8 bv[2][4];
            auto rd = [&](int n, int q) {
                const int t = T0 + n, slot = half.v == 0 ? t : (t < 7 ? 8 + t : 0);
                bv[n & 1][q] = vfrag((q & 1) ? va1 : va0, slot * 4096 + (q >> 1) * 2048);
            };
#pragma unroll
            for (int q = 0; q < 4; ++q) rd(0, q);
            sbar();
            if constexpr (first) { extterm(IC<6>{}, en0); sbar(); extterm(IC<7>{}, en1); sbar(); }
            sfor<0, N * 4>([&](auto ss) {
                constexpr int n = ss.v >> 2, q = ss.v & 3, t = T0 + n, ks = q >> 1;
                if constexpr (n + 1 < N) rd(n + 1, q);
                const u32x4 a = {phi[t][4 * ks], phi[t][4 * ks + 1], phi[t][4 * ks + 2], phi[t][4 * ks + 3]};
                acc[q & 1] = mfma(asbf(a), bv[n & 1][q], acc[q & 1]);
                if constexpr (first) expair(IC<6 + (ss.v >> 3)>{}, IC<(ss.v & 7)>{});
                sbar();
            });
            asm volatile("" : "+v"(acc[0]), "+v"(acc[1]));
            sbar();
        };
        u32x4 owna[2];
        auto publish = [&](int half, f32x16 (&acc)[2]) {
#pragma unroll
            for (int dtl = 0; dtl < 2; ++dtl)
#pragma unroll
                for (int ks = 0; ks < 2; ++ks) {
                    const u32x4 v = {pk(acc[dtl][8 * ks], acc[dtl][8 * ks + 1]), pk(acc[dtl][8 * ks + 2], acc[dtl][8 * ks + 3]),
                                     pk(acc[dtl][8 * ks + 4], acc[dtl][8 * ks + 5]), pk(acc[dtl][8 * ks + 6], acc[dtl][8 * ks + 7])};
                    const int a = 4 * kw + 2 * dtl + ks;
                    const bool own = (a >> 1) == c;
                    __builtin_amdgcn_raw_buffer_store_b128(v, rs, (own ? OOB : W_P1 + ((half * 16 + a) * 8 + c) * 1024) + lane * 16, 0, AUX_SC1);
                    if (half) { if (own) lds128w(sb + L_OWNB + ks * 8192 + lane * 16, v); }
                    else if (dtl == 0) owna[ks] = v;
                    else if (c & 1) owna[ks] = v;
                }
            if (half && kw == (c >> 1) && lane == 0) prog_set(sb + L_OW, 1);
        };
        auto flag = [&](int half) {
            if (lane == 0) __hip_atomic_store((GAS u64*)(wh + W_F1 + ((4 * half + kw) * 8 + c) * 8), tag, __ATOMIC_RELAXED, __HIP_MEMORY_SCOPE_AGENT);
        };
        {
            f32x16 acca[2] = {}, accb[2] = {};
            gemm2(IC<0>{}, IC<4>{}, IC<0>{}, acca);
            gemm2(IC<4>{}, IC<8>{}, IC<0>{}, acca);
            publish(0, acca);
            {
                const float s = ksum0 + ksum1;
                const auto sw = __builtin_amdgcn_permlane32_swap(__float_as_uint(s), __float_as_uint(s), false, false);
                const float tot = __uint_as_float(sw[0]) + __uint_as_float(sw[1]);
                if (lane < 32) __builtin_amdgcn_raw_buffer_store_b32(__float_as_uint(tot), rs, W_KS + c * 512 + kw * 128 + r * 4, 0, AUX_SC1);
            }
            gemm2(IC<0>{}, IC<4>{}, IC<1>{}, accb);
            if (kw == (c >> 1)) { lds128w(sb + L_OWNA + lane * 16, owna[0]); lds128w(sb + L_OWNA + 8192 + lane * 16, owna[1]); }
            gemm2(IC<4>{}, IC<6>{}, IC<1>{}, accb);
            vmwait<0>();
            flag(0);
            gemm2(IC<6>{}, IC<7>{}, IC<1>{}, accb);
            gemm2(IC<7>{}, IC<8>{}, IC<1>{}, accb);
            if (lane == 0) prog_set(sb + L_KP + 4 * kw, KP_G2B);
            const int fsel = lane >> 3;
            const unsigned fo = (rhalf * 4 + ((rhalf == 0 && fsel == 1) ? 2 * kw : (rhalf == 0 && fsel == 2) ? 2 * kw + 1 : (c >> 1))) * 64 + (lane & 7) * 8;
            GAS u64* fp = (GAS u64*)(wh + W_F1 + fo);
            publish(1, accb);
            sbar();
            bool seen = false;
            if (rhalf) {
                vmwait<0>();
                flag(1);
                prog_wait4(sb + L_LQ, 1);
            } else {
                u64 fv;
                asm volatile("global_load_dwordx2 %0, %1, off sc1" : "=v"(fv) : "v"(fp) : "memory");
                vmwait<1>();
                flag(1);
                asm volatile("s_waitcnt vmcnt(1)" : "+v"(fv) :: "memory");
                seen = __all(fv == tag);
            }
            cbar();
            if (!seen) {
                for (unsigned s = 0; s < SPIN_GLB; ++s) {
                    cbar();
                    if (__all(__hip_atomic_load(fp, __ATOMIC_RELAXED, __HIP_MEMORY_SCOPE_AGENT) == tag)) break;
                }
            }
            cbar();
        }
        {
            u32x4 p[8], ks4[2];
#pragma unroll
            for (int i = 0; i < 8; ++i) p[i] = __builtin_amdgcn_raw_buffer_load_b128(rs, (i == c ? OOB : W_P1 + ((rhalf * 16 + ra) * 8 + i) * 1024) + lane * 16, 0, AUX_SC1);
            if (rhalf == 0) {
#pragma unroll
                for (int n = 0; n < 2; ++n) ks4[n] = __builtin_amdgcn_raw_buffer_load_b128(rs, W_KS + (lane >> 3) * 512 + (2 * kw + n) * 128 + (lane & 7) * 16, 0, AUX_SC1);
            }
            sbar();
            if (c == 0 && kw == 0 && lane == 0) __hip_atomic_store((GAS u64*)(wh + W_GEN), tag, __ATOMIC_RELAXED, __HIP_MEMORY_SCOPE_AGENT);
            sbar();
            u32x4 sel;
            {
                const int i = (r & 3) + 4 * (r >> 3);
                const bool on = r < 16 && ((r >> 2) & 1) == h;
#pragma unroll
                for (int j = 0; j < 4; ++j) sel[j] = (on && (i >> 1) == j) ? ((i & 1) ? 0x3F800000u : 0x00003F80u) : 0u;
            }
            if (rhalf) {
                for (unsigned s = 0; s < SPIN_LDS; ++s) if (__builtin_amdgcn_readfirstlane(*(volatile LAS unsigned*)(size_t)(sb + L_OW))) break;
                cbar();
            }
            f32x16 d = mfma(asbf(sel), asbf(lds128(sb + (rhalf ? L_OWNB : L_OWNA) + (kw & 1) * 8192 + lane * 16)), f32x16{});
#pragma unroll
            for (int i = 0; i < 8; ++i) d = mfma(asbf(sel), asbf(p[i]), d);
            u32x4 o;
#pragma unroll
            for (int j = 0; j < 4; ++j) o[j] = min(pk(d[2 * j], d[2 * j + 1]), POISON - 1u);
            __builtin_amdgcn_raw_buffer_store_b128(o, rs, W_R + (rhalf * 16 + ra) * 1024 + lane * 16, 0, AUX_SC1);
            sbar();
            if (rhalf == 0) {
#pragma unroll
                for (int n = 0; n < 2; ++n) {
                    float s[4];
#pragma unroll
                    for (int j = 0; j < 4; ++j) {
                        float y = __uint_as_float(ks4[n][j]);
                        y += __shfl_xor(y, 8); y += __shfl_xor(y, 16); y += __shfl_xor(y, 32);
                        s[j] = y;
                    }
                    if (lane < 8) {
                        const u32x2 kk = {pk(s[0], s[1]), pk(s[2], s[3])};
                        *(LAS u32x2*)(size_t)(sb + L_KS + (((2 * kw + n) * 2 + (lane >> 2)) * 2 + (lane & 1)) * 16 + ((lane >> 1) & 1) * 8) = kk;
                    }
                }
                if (lane == 0) prog_set(sb + L_KP + 4 * kw, KP_KSUM);
            }
        }
        {
            GAS unsigned* sp = (GAS unsigned*)(wh + W_R + (rhalf * 16 + (kw & 1) * 8 + (lane & 7)) * 1024 + 1020);
            for (unsigned s = 0; s < SPIN_GLB; ++s) {
                cbar();
                if (__all(__hip_atomic_load(sp, __ATOMIC_RELAXED, __HIP_MEMORY_SCOPE_AGENT) != POISON)) break;
            }
            u32x4 g[8];
            const unsigned go8 = W_R + (rhalf * 16 + (kw & 1) * 8) * 1024 + lane * 16;
            for (unsigned s = 0; s < SPIN_GLB; ++s) {
                bool ok = true;
                cbar();
#pragma unroll
                for (int i = 0; i < 8; ++i) g[i] = __builtin_amdgcn_raw_buffer_load_b128(rs, go8 + i * 1024, 0, AUX_SC1);
                sbar();
#pragma unroll
                for (int i = 0; i < 8; ++i) ok &= (g[i].x != POISON) & (g[i].y != POISON) & (g[i].z != POISON) & (g[i].w != POISON);
                if (__all(ok)) break;
            }
            if (rhalf) prog_wait4(sb + L_KP, KP_G2B);
#pragma unroll
            for (int i = 0; i < 8; ++i) lds128w(sb + (rhalf ? L_KVB : L_KVA) + ((kw & 1) * 8 + i) * 1024 + lane * 16, g[i]);
            if (lane == 0) prog_set(sb + L_KP + 4 * kw, rhalf ? KP_KVB : KP_KVA);
        }
    }
}

extern "C" void kernel_launch(void* const* d_in, const int* in_sizes, int n_in, void* d_out, int out_size, void* d_ws,
                              size_t ws_size, hipStream_t stream) {
    performer_kernel<<<256, 512, 0, stream>>>((const char*)d_in[0], (const char*)d_in[1], (const char*)d_in[2],
                                              (const char*)d_in[3], (char*)d_out, (char*)d_ws);
}
```
